```python
import torch

def _softmax(x, axis=-1):
    return torch.softmax(x, dim=axis)

def _gelu(x):
    return torch.nn.functional.gelu(x, approximate='tanh')

def _silu(x):
    return torch.nn.functional.silu(x)

def _sigmoid(x):
    return torch.sigmoid(x)

def _layernorm(x, axis=-1, eps=1e-05):
    m = x.mean(dim=axis, keepdim=True)
    v = x.var(dim=axis, keepdim=True, unbiased=False)
    return (x - m) / torch.sqrt(v + eps)

def _scatter_add(tgt, idx, src, axis=0):
    return tgt.clone().index_add_(axis, idx, src)

def _segment_sum(src, seg, n):
    out = torch.zeros((n,) + src.shape[1:], dtype=src.dtype)
    return out.index_add_(0, seg, src)

def _pad(x, pw):
    flat = tuple((v for pair in reversed(pw) for v in pair))
    return torch.nn.functional.pad(x, flat)
kernel_name = 'interleave_layers__memory_aug_recurrent_mem_tr__glu_gated_reglu__prompt'

def setup_inputs():

    _w = lambda a: (lambda t: t.to(torch.bfloat16) if t.is_floating_point() else t)(torch.as_tensor(a))

    def _A_setup():
        B, S, Dm = (1, 2048, 512)
        H, Dh = (32, 128)
        M, seg = (8, 32)
        seg = min(seg, S)
        S = S // seg * seg
        return {'x': _w((lambda *s: torch.randn(*s, dtype=torch.bfloat16) * 0.05)(B, S, Dm)), 'mem0': _w((lambda *s: torch.randn(*s, dtype=torch.bfloat16) * 0.2)(B, M, Dm)), 'Wq': _w((lambda *s: torch.randn(*s, dtype=torch.bfloat16) * 1.6)(Dm, H, Dh) * Dm ** (-0.5)), 'Wk': _w((lambda *s: torch.randn(*s, dtype=torch.bfloat16) * 1.6)(Dm, H, Dh) * Dm ** (-0.5)), 'Wv': _w((lambda *s: torch.randn(*s, dtype=torch.bfloat16) * 0.2)(Dm, H, Dh) * Dm ** (-0.5)), 'Wo': _w((lambda *s: torch.randn(*s, dtype=torch.bfloat16) * 0.2)(H, Dh, Dm) * (H * Dh) ** (-0.5)), 'seg_len': seg}
    _A_in = _A_setup()

    def _B_setup():
        B, S, D, F = (1, 2048, 512, 1792)
        return {'x': _w((lambda *s: torch.randn(*s, dtype=torch.bfloat16) * 0.05)(B, S, D)), 'W_gate': _w((lambda *s: torch.randn(*s, dtype=torch.bfloat16) * 0.2)(D, F) * (2.0 / D) ** 0.5), 'W_up': _w((lambda *s: torch.randn(*s, dtype=torch.bfloat16) * 0.2)(D, F) * (2.0 / D) ** 0.5), 'W_down': _w((lambda *s: torch.randn(*s, dtype=torch.bfloat16) * 0.2)(F, D) * (1.0 / F) ** 0.5)}
    _B_in = _B_setup()
    return {'A_x': _A_in['x'], 'A_mem0': _A_in['mem0'], 'A_Wq': _A_in['Wq'], 'A_Wk': _A_in['Wk'], 'A_Wv': _A_in['Wv'], 'A_Wo': _A_in['Wo'], 'A_seg_len': _A_in['seg_len'], 'B_x': _B_in['x'], 'B_W_gate': _B_in['W_gate'], 'B_W_up': _B_in['W_up'], 'B_W_down': _B_in['W_down']}

def reference(A_x, A_mem0, A_Wq, A_Wk, A_Wv, A_Wo, A_seg_len, B_x, B_W_gate, B_W_up, B_W_down):

    def _A_ref(x, mem0, Wq, Wk, Wv, Wo, seg_len):
        B, S, Dm = x.shape
        H, Dh = (Wq.shape[1], Wq.shape[2])
        M = mem0.shape[1]
        scale = Dh ** (-0.5)
        n_seg = S // seg_len
        L = M + seg_len
        idx = torch.arange(L)
        mask = (idx[:, None] < M) | (idx[:, None] >= idx[None, :])
        mem = mem0
        outs = []
        for s in range(n_seg):
            xs = x[:, s * seg_len:(s + 1) * seg_len, :].clone()
            h = torch.concatenate([mem, xs], axis=1)
            q = torch.einsum('btd,dhe->bhte', h, Wq)
            k = torch.einsum('btd,dhe->bhte', h, Wk)
            v = torch.einsum('btd,dhe->bhte', h, Wv)
            scores = torch.einsum('bhqe,bhke->bhqk', q, k) * scale
            scores = torch.where(mask[None, None], scores, -1000000000.0)
            p = _softmax(scores, axis=-1)
            o = torch.einsum('bhqk,bhke->bhqe', p, v)
            o = torch.einsum('bhte,hed->btd', o, Wo)
            mem = o[:, :M, :].clone()
            outs.append(o[:, M:, :])
        return torch.concatenate(outs, axis=1)

    def _B_ref(x, W_gate, W_up, W_down):
        gate = torch.einsum('bsd,df->bsf', x, W_gate)
        up = torch.einsum('bsd,df->bsf', x, W_up)
        gate = gate * (lambda x, dt: x.to(dt))(gate > 0, gate.dtype)
        h = gate * up
        return torch.einsum('bsf,fd->bsd', h, W_down)
    x = A_x
    x = x + _A_ref(_layernorm(x, axis=-1), A_mem0, A_Wq, A_Wk, A_Wv, A_Wo, A_seg_len)
    x = x + _B_ref(_layernorm(x, axis=-1), B_W_gate, B_W_up, B_W_down)
    x = x + _A_ref(_layernorm(x, axis=-1), A_mem0, A_Wq, A_Wk, A_Wv, A_Wo, A_seg_len)
    x = x + _B_ref(_layernorm(x, axis=-1), B_W_gate, B_W_up, B_W_down)
    return x
```

```cpp
#include <hip/hip_runtime.h>
#include <hip/hip_bf16.h>
#if defined(__HIP_DEVICE_COMPILE__) && __HIP_DEVICE_COMPILE__
#pragma clang attribute push(__attribute__((target("no-packed-fp32-ops"))), apply_to = function)
#define NOPK_PUSHED 1
#endif

typedef __attribute__((ext_vector_type(8))) __bf16 bf16x8;
typedef __attribute__((ext_vector_type(16))) __bf16 bf16x16;
typedef __attribute__((ext_vector_type(2))) __bf16 bf16x2;
typedef __attribute__((ext_vector_type(8))) unsigned u32x8;
typedef __attribute__((ext_vector_type(4))) float f32x4;
typedef __attribute__((ext_vector_type(2))) float f32x2;
typedef __attribute__((ext_vector_type(4))) unsigned u32x4;
typedef __attribute__((ext_vector_type(2))) unsigned u32x2;
typedef __attribute__((ext_vector_type(4))) short s16x4;
#define LAS __attribute__((address_space(3)))
#define DEV __device__ __forceinline__
typedef __amdgpu_buffer_rsrc_t rsrc_t;
typedef LAS unsigned char* lds_t;

constexpr int NSEG = 64, DM = 512, NH = 32, FF = 1792;
#define AHEAD_ 4
#define MAP 6
constexpr int AHEAD = AHEAD_;
constexpr int URING = 8;
constexpr unsigned MEM_SLOT = 8192, PM_SLOT = NH * 640, UF_SLOT = 512, X_SLOT = 32768, PX_SLOT = NH * 2560, H_SLOT = 114688,
                   Y_SLOT = 262144, U_SLOT = 1048576;
constexpr unsigned WS_MEM = 0;
constexpr unsigned WS_PM = WS_MEM + 2 * (NSEG + 1) * MEM_SLOT;
constexpr unsigned WS_UF = WS_PM + 2 * NSEG * PM_SLOT;
constexpr unsigned WS_SMALL_END = WS_UF + 2 * NSEG * UF_SLOT;
constexpr unsigned WS_X = 4u << 20;
constexpr unsigned WS_X1 = WS_X + 2 * NSEG * X_SLOT;
constexpr unsigned WS_PX = WS_X1 + 2 * NSEG * X_SLOT;
constexpr unsigned WS_H = WS_PX + 2 * NSEG * PX_SLOT;
constexpr unsigned WS_Y = WS_H + 2 * NSEG * H_SLOT;
constexpr unsigned WS_U = WS_Y + 2 * NSEG * Y_SLOT;
constexpr unsigned WS_XN = WS_U + 2 * URING * U_SLOT;
constexpr unsigned WS_END = WS_XN + 2 * NSEG * X_SLOT;
static_assert(WS_SMALL_END <= WS_X, "ws map");
constexpr unsigned RS_END = WS_END;
constexpr int LDS_BYTES = 144 * 1024;
constexpr unsigned SENT = 0xFFFFFFFFu;
constexpr int SPIN_BUDGET = 1 << 20;
#define R_DELAY 0
#define Y_DELAY 60
#define V_PACE 0
#define U_NAP 0
#define LATE_DMA 0
#define BELL 3
#define Y_EXTRA 4
#define UGATE 60
#define S_PACE 0

struct Params {
  const __hip_bfloat16 *x, *Wq, *Wk, *Wv, *Wo, *Wg, *Wu, *Wd;
  __hip_bfloat16* out;
  unsigned char* ws;
};

#define ST(s, i)
#define STW(s, i, w)
#define TK_DECL
#define TK(i)
#define TKC(i)
#define TK_DUMP(id)
DEV unsigned f2u(float x) { return __builtin_bit_cast(unsigned, x); }
DEV float u2f(unsigned x) { return __builtin_bit_cast(float, x); }
DEV bool any_lane(bool x) { return __builtin_amdgcn_ballot_w64(x) != 0; }
DEV unsigned umin(unsigned a, unsigned b) { return a < b ? a : b; }
DEV unsigned umax(unsigned a, unsigned b) { return a > b ? a : b; }
DEV float fmaxn(float a, float b) { return __builtin_elementwise_maximum(a, b); }
DEV unsigned opaque(unsigned x) { asm volatile("" : "+v"(x)); return x; }
DEV f32x4 zero4() { return (f32x4){0.f, 0.f, 0.f, 0.f}; }
DEV u32x4 sent4() { return (u32x4){SENT, SENT, SENT, SENT}; }
DEV f32x4 mfma(u32x4 a, u32x4 b, f32x4 c) {
  return __builtin_amdgcn_mfma_f32_16x16x32_bf16(__builtin_bit_cast(bf16x8, a), __builtin_bit_cast(bf16x8, b), c, 0, 0, 0);
}
DEV f32x4 smfmac(u32x4 a, u32x8 b, f32x4 c, int idx) {
  return __builtin_amdgcn_smfmac_f32_16x16x64_bf16(__builtin_bit_cast(bf16x8, a), __builtin_bit_cast(bf16x16, b), c, idx, 0, 0);
}
DEV u32x4 half(const u32x8& v, int h) { return h ? __builtin_shufflevector(v, v, 4, 5, 6, 7) : __builtin_shufflevector(v, v, 0, 1, 2, 3); }
DEV u32x8 join(u32x4 a, u32x4 b) { return __builtin_shufflevector(a, b, 0, 1, 2, 3, 4, 5, 6, 7); }
DEV u32x4 join2(u32x2 a, u32x2 b) { return __builtin_shufflevector(a, b, 0, 1, 2, 3); }
DEV f32x4 fold32(f32x4 a, f32x4 b) {
  f32x4 o;
  _Pragma("unroll") for (int j = 0; j < 4; ++j) {
    auto t = __builtin_amdgcn_permlane32_swap(f2u(a[j]), f2u(b[j]), false, false);
    o[j] = u2f(t[0]) + u2f(t[1]);
  }
  return o;
}
DEV float ror8(float x) { return u2f(__builtin_amdgcn_update_dpp(0, f2u(x), 0x128, 0xf, 0xf, true)); }
DEV unsigned pk(float lo, float hi) { bf16x2 v = {(__bf16)lo, (__bf16)hi}; return __builtin_bit_cast(unsigned, v); }
DEV float bflo(unsigned u) { return u2f(u << 16); }
DEV float bfhi(unsigned u) { return u2f(u & 0xffff0000u); }
DEV float dot2(unsigned a, unsigned b, float c) {
  return __builtin_amdgcn_fdot2_f32_bf16(__builtin_bit_cast(bf16x2, a), __builtin_bit_cast(bf16x2, b), c, false);
}
DEV u32x4 ldg(rsrc_t r, unsigned voff, unsigned soff) { return __builtin_amdgcn_raw_buffer_load_b128(r, voff, soff, 16); }
DEV void stg(rsrc_t r, unsigned voff, unsigned soff, u32x4 v) {
  __builtin_amdgcn_raw_buffer_store_b128(v, r, voff, soff, 16);
  asm volatile("s_nop 1" ::"v"(v) : "memory");
}
DEV u32x4 unsent(u32x4 v) {
  v.x = umin(v.x, SENT - 1); v.y = umin(v.y, SENT - 1); v.z = umin(v.z, SENT - 1); v.w = umin(v.w, SENT - 1); return v;
}
DEV u32x4 lr128(lds_t l, unsigned off) { return *(LAS u32x4*)(l + off); }
DEV f32x4 lr128f(lds_t l, unsigned off) { return *(LAS f32x4*)(l + off); }
DEV void lw128(lds_t l, unsigned off, u32x4 v) { *(LAS u32x4*)(l + off) = v; }
DEV void lw128f(lds_t l, unsigned off, f32x4 v) { *(LAS f32x4*)(l + off) = v; }
DEV void lw64(lds_t l, unsigned off, u32x2 v) { *(LAS u32x2*)(l + off) = v; }
DEV u32x2 lds_tr(lds_t l, unsigned off) {
  return __builtin_bit_cast(u32x2, __builtin_amdgcn_ds_read_tr16_b64_v4i16((LAS s16x4*)(l + off)));
}
DEV unsigned now() { return (unsigned)__builtin_amdgcn_s_memrealtime(); }
DEV void wait_until(unsigned t) { for (int i = 0; i < 4096 && (int)(now() - t) < 0; ++i) {} asm volatile("" ::: "memory"); }
DEV void drain() { asm volatile("s_waitcnt vmcnt(0)" ::: "memory"); }
DEV void bar() {
  __builtin_amdgcn_fence(__ATOMIC_RELEASE, "workgroup", "local");
  __builtin_amdgcn_s_barrier();
  __builtin_amdgcn_fence(__ATOMIC_ACQUIRE, "workgroup", "local");
}
#define SB() __builtin_amdgcn_sched_barrier(0)
#define UNR _Pragma("unroll")
DEV float xmax(float x) {
  auto s = __builtin_amdgcn_permlane16_swap(f2u(x), f2u(x), false, false);
  x = fmaxn(u2f(s[0]), u2f(s[1]));
  auto t = __builtin_amdgcn_permlane32_swap(f2u(x), f2u(x), false, false);
  return fmaxn(u2f(t[0]), u2f(t[1]));
}
DEV float xsum(float x) {
  auto s = __builtin_amdgcn_permlane16_swap(f2u(x), f2u(x), false, false);
  x = u2f(s[0]) + u2f(s[1]);
  auto t = __builtin_amdgcn_permlane32_swap(f2u(x), f2u(x), false, false);
  return u2f(t[0]) + u2f(t[1]);
}
DEV u32x4 pair16(u32x2 a, u32x2 b) {
  auto r0 = __builtin_amdgcn_permlane16_swap(a.x, b.x, false, false);
  auto r1 = __builtin_amdgcn_permlane16_swap(a.y, b.y, false, false);
  u32x4 v = {r0[0], r1[0], r0[1], r1[1]};
  return v;
}
DEV u32x2 pk4(f32x4 a) { u32x2 v = {pk(a[0], a[1]), pk(a[2], a[3])}; return v; }
DEV u32x4 add_bf8(u32x4 a, u32x4 b) {
  u32x4 r;
  UNR for (int i = 0; i < 4; ++i) r[i] = pk(bflo(a[i]) + bflo(b[i]), bfhi(a[i]) + bfhi(b[i]));
  return r;
}
DEV f32x4 sum8(lds_t l, unsigned off, unsigned stride) {
  f32x4 t[8];
  UNR for (int w = 0; w < 8; ++w) t[w] = lr128f(l, off + w * stride);
  return ((t[0] + t[1]) + (t[2] + t[3])) + ((t[4] + t[5]) + (t[6] + t[7]));
}
template <int N>
DEV bool all_there(const u32x4 (&v)[N]) {
  unsigned m = 0;
  UNR for (int i = 0; i < N; ++i) m = umax(umax(m, umax(v[i].x, v[i].y)), umax(v[i].z, v[i].w));
  return !any_lane(m == SENT);
}
#define POLL(v, SLEEP, ISSUE)                                   \
  for (;;) {                                                    \
    ISSUE;                                                      \
    const bool ok_ = all_there(v);                              \
    asm volatile("" ::: "memory");                              \
    if (ok_ || --budget < 0) break;                             \
    if (SLEEP) __builtin_amdgcn_s_sleep(SLEEP);                 \
  }

DEV unsigned swz8(unsigned row) { return (row & 3) | ((row >> 1) & 4); }
template <int ROWB>
DEV void wblk_load(u32x4 (&st)[16], const __hip_bfloat16* base, unsigned ld, int tid) {
  constexpr unsigned CPR = ROWB / 16;
  UNR for (int i = 0; i < 16; ++i) {
    const unsigned n = i * 512 + tid, row = n / CPR, ch = n % CPR;
    st[i] = *(const u32x4*)(base + (size_t)row * ld + ch * 8);
  }
}
template <int ROWB>
DEV void wblk_put(lds_t l, const u32x4 (&st)[16], int tid) {
  constexpr unsigned CPR = ROWB / 16;
  unsigned base[2];
  UNR for (int i = 0; i < 2; ++i) {
    const unsigned n = i * 512 + tid, row = n / CPR, ch = n % CPR;
    base[i] = opaque(row * ROWB + ((ch ^ (swz8(row) << 1)) << 4));
  }
  UNR for (int i = 0; i < 8; ++i) lw128(l, base[i & 1] + (i >> 1) * 16384, st[i]);
  UNR for (int i = 0; i < 2; ++i) base[i] = opaque(base[i] + 65536);
  UNR for (int i = 8; i < 16; ++i) lw128(l, base[i & 1] + ((i - 8) >> 1) * 16384, st[i]);
}
template <int ROWB>
DEV unsigned wblk_base(unsigned c16, int lane) {
  const unsigned r0 = 8 * (lane >> 4) + ((lane & 15) >> 2);
  return opaque(r0 * ROWB + ((c16 ^ swz8(r0)) << 5) + (lane & 3) * 8);
}
template <int ROWB>
DEV u32x8 wblk_pair(lds_t l, unsigned base, int kp) {
  constexpr unsigned KP = 64 * ROWB;
  const unsigned hi = (kp * KP) & ~65535u, o = kp * KP - hi;
  const unsigned b = hi ? base + hi : base;
  return join(join2(lds_tr(l, b + o), lds_tr(l, b + o + 4 * ROWB)), join2(lds_tr(l, b + o + 32 * ROWB), lds_tr(l, b + o + 36 * ROWB)));
}
DEV void load_w2(u32x8 (&w)[16], lds_t l, const __hip_bfloat16* w0, const __hip_bfloat16* w1, unsigned ld, int tid, int wave, int lane) {
  u32x4 s0[16], s1[16];
  wblk_load<256>(s0, w0, ld, tid);
  wblk_load<256>(s1, w1, ld, tid);
  wblk_put<256>(l, s0, tid); bar();
  const unsigned wb = wblk_base<256>(wave, lane);
  UNR for (int i = 0; i < 8; ++i) w[i] = wblk_pair<256>(l, wb, i);
  bar(); wblk_put<256>(l, s1, tid); bar();
  UNR for (int i = 0; i < 8; ++i) w[8 + i] = wblk_pair<256>(l, wb, i);
  bar();
}
DEV u32x4 ones4() { return (u32x4){0x3F803F80u, 0x3F803F80u, 0x3F803F80u, 0x3F803F80u}; }

constexpr unsigned L_MEM = 0, L_ST = 8192, L_FS = 12288, L_M0 = 14336, L_T = 18432, L_XS = 65536;
#define X_WAIT(v, soff)                                                                     \
  { u32x4 p_[1]; POLL(p_, 4, p_[0] = ldg(r, l16 + 3072, (soff) + wave * 4096)); POLL(v, 2, X_ISSUE(v, soff)); }
#define X_ISSUE(v, soff) UNR for (int i_ = 0; i_ < 4; ++i_) v[i_] = ldg(r, l16 + i_ * 1024, (soff) + wave * 4096)
DEV void x_part(lds_t l, int buf, const u32x4 (&v)[4], int wave, unsigned l16) {
  float s1 = 0.f, s2 = 0.f;
  UNR for (int i = 0; i < 4; ++i)
    UNR for (int j = 0; j < 4; ++j) { s1 = dot2(v[i][j], 0x3F803F80u, s1); s2 = dot2(v[i][j], v[i][j], s2); }
  asm volatile("s_nop 3" : "+v"(s1), "+v"(s2));
  auto a = __builtin_amdgcn_permlane32_swap(f2u(s1), f2u(s2), false, false);
  const float t = u2f(a[0]) + u2f(a[1]);
  *(LAS float*)(l + (L_ST + buf * 2048 + (wave >> 1) * 512 + (wave & 1) * 8) + ((l16 & 0x1f0) + ((l16 >> 9) << 2))) = t;
}
DEV void x_put(lds_t l, int buf, const u32x4 (&v)[4], int wave, unsigned l16) {
  const unsigned wb = opaque(l16 + (L_XS + buf * 32768 + wave * 4096));
  UNR for (int i = 0; i < 4; ++i) lw128(l, wb + i * 1024, v[i]);
  x_part(l, buf, v, wave, l16);
}
DEV void x_rowstats(lds_t l, int buf, unsigned l16, float& mean, float& rstd) {
  const unsigned o = opaque((l16 & 0x1f0) + (l16 >> 9) * 1024 + buf * 2048);
  const f32x4 a = lr128f(l, L_ST + o) + lr128f(l, L_ST + o + 512);
  auto t = __builtin_amdgcn_permlane32_swap(f2u(a[0] + a[2]), f2u(a[1] + a[3]), false, false);
  const float u = u2f(t[0]) + u2f(t[1]);
  auto z = __builtin_amdgcn_permlane32_swap(f2u(u), f2u(u), false, false);
  mean = u2f(z[0]) * (1.f / DM);
  rstd = __builtin_amdgcn_rsqf(__builtin_fmaxf(u2f(z[1]) * (1.f / DM) - mean * mean, 0.f) + 1e-5f);
}
DEV void x_final(lds_t l, int buf, int wave, unsigned l16) {
  float mean, rstd;
  x_rowstats(l, buf, l16, mean, rstd);
  *(LAS float*)(l + L_FS + wave * 256 + (l16 >> 2)) = l16 < 512 ? rstd : -mean * rstd;
}
template <bool WB>
DEV void x_gemm(lds_t l, int buf, unsigned a32, const u32x8 (&w)[16], f32x4 (&a0)[2], f32x4 (&a1)[2]) {
  const unsigned xb = opaque(a32 + (L_XS + buf * 32768));
  u32x4 f[2][4];
  UNR for (int i = 0; i < 4; ++i) f[0][i] = lr128(l, xb + (i >> 1) * 2048 + (i & 1) * 256);
  UNR for (int gr = 0; gr < 8; ++gr) {
    if (gr + 1 < 8) {
      UNR for (int i = 0; i < 4; ++i) f[(gr + 1) & 1][i] = lr128(l, xb + ((gr + 1) * 2 + (i >> 1)) * 2048 + (i & 1) * 256);
    }
    SB();
    UNR for (int i = 0; i < 4; ++i) {
      const u32x4 x = f[gr & 1][i], w0 = half(w[gr], i >> 1), w1 = half(w[8 + gr], i >> 1);
      a0[i & 1] = WB ? mfma(x, w0, a0[i & 1]) : mfma(w0, x, a0[i & 1]);
      a1[i & 1] = WB ? mfma(x, w1, a1[i & 1]) : mfma(w1, x, a1[i & 1]);
    }
    SB();
  }
}
template <bool WB>
DEV f32x4 colsum(const u32x8 (&w)[16], int set) {
  f32x4 c = zero4();
  UNR for (int i = 0; i < 16; ++i) c = WB ? mfma(ones4(), half(w[8 * set + (i >> 1)], i & 1), c) : mfma(half(w[8 * set + (i >> 1)], i & 1), ones4(), c);
  return c;
}
DEV unsigned timg_w(unsigned k, unsigned g) { return k * 64 + ((g ^ ((k >> 1) & 7)) << 3); }
DEV unsigned timg_r(int lane, int hf) {
  const unsigned k = 8 * (lane >> 4) + 4 * hf + ((lane & 15) >> 2);
  return k * 64 + (((lane & 3) ^ ((k >> 1) & 7)) << 3);
}
DEV unsigned flag_off(int L, int row, int i) { return WS_UF + ((L * 8 + row) * 32 + i) * 128; }
DEV int flag_ld(rsrc_t r, unsigned off) { return (int)__builtin_amdgcn_raw_buffer_load_b32(r, 0, off, 16); }
DEV void bell(rsrc_t r, int L, int row, int h, unsigned t, int s, int lane) {
  if (lane == 0) __builtin_amdgcn_raw_buffer_store_b32((t << 6) | (unsigned)s, r, 0, flag_off(L, row, h), 16);
}
DEV void xn_fetch(rsrc_t r, lds_t l, int L, int sx, int f, int wave, unsigned l16, int& budget) {
  while (f < sx && --budget >= 0) {
    f = __builtin_amdgcn_readfirstlane(flag_ld(r, flag_off(L, 4 + (sx & 1), wave)));
    asm volatile("" ::: "memory");
    if (f < sx) __builtin_amdgcn_s_sleep(4);
  }
  const unsigned dst = (unsigned)(size_t)l + L_XS + (sx & 1) * 32768 + wave * 4096, src = WS_XN + (L * NSEG + sx) * X_SLOT + wave * 4096;
  unsigned keep;
  asm volatile("s_mov_b32 %0, m0\n\ts_mov_b32 m0, %1\n\ts_nop 4\n\t"
               "buffer_load_dwordx4 %2, %3, %4 offen sc1 lds\n\tbuffer_load_dwordx4 %2, %3, %4 offen offset:1024 sc1 lds\n\t"
               "buffer_load_dwordx4 %2, %3, %4 offen offset:2048 sc1 lds\n\tbuffer_load_dwordx4 %2, %3, %4 offen offset:3072 sc1 lds\n\t"
               "s_mov_b32 m0, %0"
               : "=&s"(keep) : "s"(dst), "v"(l16), "s"(r), "s"(src) : "memory");
}
#ifndef M_SLEEP
#define M_SLEEP 0
#endif
#define MEM_STAGE(s)                                                                                      \
  { u32x4 m_[1]; POLL(m_, M_SLEEP, m_[0] = ldg(r, l16, WS_MEM + (L * (NSEG + 1) + (s)) * MEM_SLOT + wave * 1024)); \
    lw128(l, L_MEM + wave * 1024 + l16, m_[0]); }
DEV int simd_id() { return (__builtin_amdgcn_s_getreg(4 | (4 << 6) | (1 << 11))) & 3; }
#define S_NAP 8
#define V_NAP 8

template <int KIND>
DEV void role_S(const u32x8 (&w)[16], rsrc_t r, lds_t l, int L, int h, int tid, int wave, int lane) {
  constexpr unsigned L_KX = L_T, L_QX = L_T + 16384;
  int budget = SPIN_BUDGET;
  TK_DECL
  if (tid == 0) *(LAS int*)(l + L_M0) = simd_id();
  const int c = lane & 15, g = lane >> 4;
  const unsigned l16 = opaque(lane * 16), a32 = opaque((g * 32 + c) * 16);
  const int sidx = (int)opaque(c < 8 ? 0x4444u : 0xEEEEu);
  const unsigned tw = opaque(timg_w(16 * wave + c, g)), tr0 = opaque(timg_r(lane, 0)), tr1 = opaque(timg_r(lane, 1));
  const unsigned mw = opaque(L_M0 + (16 * wave + c) * 32 + ((g ^ (c >> 2)) << 3));
  const unsigned mr0 = opaque(L_M0 + (8 * g + (c >> 2)) * 32 + (((c & 1) ^ (2 * g & 3)) << 3));
  const unsigned mr1 = opaque(L_M0 + (8 * g + 4 + (c >> 2)) * 32 + (((c & 1) ^ ((2 * g + 1) & 3)) << 3));
  const unsigned mk0 = opaque(mr0 ^ 16), mk1 = opaque(mr1 ^ 16);
  const unsigned po0 = opaque((g * 8 + (c & 7)) * 16 + (c >> 3) * 8), po1 = opaque(512 + c * 16 + (g & 1) * 8 + (g >> 1) * 4);
  const int lim = 16 * (wave - 1) + c - 4 * g;
  auto x_phase = [&](int sx) {
    const int b = sx & 1;
    f32x4 aq[2] = {zero4(), zero4()}, ak[2] = {zero4(), zero4()};
    x_gemm<true>(l, b, a32, w, aq, ak);
    TK(7);
    UNR for (int t = 0; t < 2; ++t) {
      const unsigned o = opaque((tw ^ (t * 32)) + b * 8192);
      lw64(l, L_QX + o, pk4(aq[t]));
      lw64(l, L_KX + o, pk4(ak[t]));
    }
    TK(8);
  };
  xn_fetch(r, l, L, 0, -1, wave, l16, budget);
  xn_fetch(r, l, L, 1, -1, wave, l16, budget);
  drain();
  bar();
  const bool napper = wave != 0 && __builtin_amdgcn_readfirstlane(*(LAS int*)(l + L_M0)) == simd_id();
  x_phase(0);
  constexpr float SC = 0.08838834764831845f * 1.4426950408889634f, NEG = -1.0e9f / 0.08838834764831845f;
  unsigned tref = 0, tb = 0, dur = 0;
  for (int s = 0; s < NSEG; ++s) {
    ST(s, 0);
    const unsigned k0 = opaque(tr0 + (s & 1) * 8192), k1 = opaque(tr1 + (s & 1) * 8192), k2 = opaque(k0 ^ 32), k3 = opaque(k1 ^ 32);
    const int xfv = flag_ld(r, flag_off(L, 4 + (s & 1), wave));
    if (S_PACE && s) wait_until(tref + S_PACE);
    TK(11);
    MEM_STAGE(s);
    const int xf = __builtin_amdgcn_readfirstlane(xfv);
    TK(0);
    bar();
    TK(1);
    if (S_PACE) tref = now();
    ST(s, 1);
    u32x4 f0[4], f1[4];
    {
      f32x4 aq = zero4(), ak = zero4();
      u32x4 f[8];
      if (BELL && KIND == 0) tb = now();
      UNR for (int i = 0; i < 8; ++i) f[i] = lr128(l, L_MEM + l16 + i * 1024);
      if (BELL && KIND == 0) bell(r, L, 6, h, tb + dur, s, lane);
      SB();
      UNR for (int i = 0; i < 4; ++i) { aq = smfmac(f[i], w[i], aq, sidx); ak = smfmac(f[i], w[8 + i], ak, sidx); }
      SB();
      if (KIND < 2) {
        UNR for (int ks = 0; ks < 4; ++ks) {
          f0[ks] = join2(lds_tr(l, L_KX + k0 + ks * 2048), lds_tr(l, L_KX + k1 + ks * 2048));
          f1[ks] = join2(lds_tr(l, L_KX + k2 + ks * 2048), lds_tr(l, L_KX + k3 + ks * 2048));
        }
      }
      SB();
      UNR for (int i = 4; i < 8; ++i) { aq = smfmac(f[i], w[i], aq, sidx); ak = smfmac(f[i], w[8 + i], ak, sidx); }
      lw64(l, mw, pk4(fold32(aq, ak)));
    }
    TK(2);
    bar();
    TK(3);
    ST(s, 2);
    if (KIND == 0) {
      __builtin_amdgcn_s_setprio(3);
      f32x4 am = zero4(), a0 = am, a1 = am;
      u32x4 fq[4], fm[4];
      UNR for (int ks = 0; ks < 4; ++ks) fq[ks] = join2(lds_tr(l, mr0 + ks * 1024), lds_tr(l, mr1 + ks * 1024));
      UNR for (int ks = 0; ks < 4; ++ks) fm[ks] = join2(lds_tr(l, mk0 + ks * 1024), lds_tr(l, mk1 + ks * 1024));
      SB();
      UNR for (int ks = 0; ks < 4; ++ks) { a0 = mfma(f0[ks], fq[ks], a0); a1 = mfma(f1[ks], fq[ks], a1); }
      UNR for (int ks = 0; ks < 4; ++ks) am = mfma(fm[ks], fq[ks], am);
      f32x4 e = c < 8 ? a0 : a1;
      float em = g < 2 ? (c < 8 ? am[0] : am[1]) : (c < 8 ? am[2] : am[3]);
      float mx = fmaxn(fmaxn(fmaxn(e[0], e[1]), e[2]), fmaxn(e[3], em));
      mx = xmax(fmaxn(mx, ror8(mx))) * -SC;
      UNR for (int j = 0; j < 4; ++j) e[j] = __builtin_amdgcn_exp2f(__builtin_fmaf(e[j], SC, mx));
      em = __builtin_amdgcn_exp2f(__builtin_fmaf(em, SC, mx));
      float sum = ((e[0] + e[1]) + (e[2] + e[3])) + em;
      const float inv = __builtin_amdgcn_rcpf(xsum(sum + ror8(sum)));
      u32x2 px = pk4(e * inv);
      em *= inv;
      const unsigned pm = umin(pk(em, ror8(em)), SENT - 1);
      px.x = umin(px.x, SENT - 1); px.y = umin(px.y, SENT - 1);
      const unsigned o = WS_PM + (L * NSEG + s) * PM_SLOT + h * 640;
      __builtin_amdgcn_raw_buffer_store_b64(px, r, po0, o, 16);
      if (c < 8) __builtin_amdgcn_raw_buffer_store_b32(pm, r, po1, o, 16);
      if (BELL) dur = now() - tb;
      __builtin_amdgcn_s_setprio(0);
    } else if (KIND == 1) {
      f32x4 am = zero4(), a0 = am, a1 = am;
      u32x4 fq[4], fm[4];
      const unsigned x = (wave - 1) * 32;
      UNR for (int ks = 0; ks < 4; ++ks) {
        fq[ks] = join2(lds_tr(l, L_QX + (k0 ^ x) + ks * 2048), lds_tr(l, L_QX + (k1 ^ x) + ks * 2048));
        fm[ks] = join2(lds_tr(l, mk0 + ks * 1024), lds_tr(l, mk1 + ks * 1024));
      }
      SB();
      UNR for (int ks = 0; ks < 4; ++ks) { a0 = mfma(f0[ks], fq[ks], a0); a1 = mfma(f1[ks], fq[ks], a1); am = mfma(fm[ks], fq[ks], am); }
      UNR for (int j = 0; j < 4; ++j) { a0[j] = j <= lim ? a0[j] : NEG; a1[j] = j + 16 <= lim ? a1[j] : NEG; }
      float mx = fmaxn(fmaxn(fmaxn(a0[0], a0[1]), fmaxn(a0[2], a0[3])), fmaxn(fmaxn(a1[0], a1[1]), fmaxn(a1[2], a1[3])));
      mx = xmax(fmaxn(fmaxn(mx, fmaxn(am[0], am[1])), fmaxn(am[2], am[3]))) * -SC;
      UNR for (int j = 0; j < 4; ++j) {
        a0[j] = __builtin_amdgcn_exp2f(__builtin_fmaf(a0[j], SC, mx)); a1[j] = __builtin_amdgcn_exp2f(__builtin_fmaf(a1[j], SC, mx));
        am[j] = __builtin_amdgcn_exp2f(__builtin_fmaf(am[j], SC, mx));
      }
      const float sm = (am[0] + am[1]) + (am[2] + am[3]);
      const float sum = ((a0[0] + a0[1]) + (a0[2] + a0[3])) + ((a1[0] + a1[1]) + (a1[2] + a1[3])) + (g < 2 ? sm : 0.f);
      const float inv = __builtin_amdgcn_rcpf(xsum(sum));
      const u32x2 pmm = pk4(am * inv);
      const unsigned o = WS_PX + (L * NSEG + s) * PX_SLOT + h * 2560 + (wave - 1) * 1280;
      stg(r, l16, o, unsent(join2(pk4(a0 * inv), pk4(a1 * inv))));
      const u32x4 vm = unsent(pair16(pmm, pmm));
      if (g == 0) stg(r, l16 + 1024, o, vm);
      if (s + AHEAD < NSEG) {
        stg(r, l16, o + AHEAD * PX_SLOT, sent4());
        if (g == 0) stg(r, l16 + 1024, o + AHEAD * PX_SLOT, sent4());
      }
    }
    ST(s, 3);
    TK(4);
    if (!LATE_DMA && s + 2 < NSEG) xn_fetch(r, l, L, s + 2, xf, wave, l16, budget);
    if (KIND == 2 && napper) __builtin_amdgcn_s_sleep(S_NAP);
    TK(5);
    if (s + 1 < NSEG) x_phase(s + 1);
    if (LATE_DMA && s + 2 < NSEG) xn_fetch(r, l, L, s + 2, xf, wave, l16, budget);
    ST(s, 4);
    STW(s, 5, 4); STW(s, 6, 7);
  }
  TK_DUMP(h);
}

template <bool isv>
DEV void role_V(const Params& p, rsrc_t r, lds_t l, int L, int h, int tid, int wave, int lane) {
  constexpr unsigned L_VM = L_M0, L_VX = L_T;
  int budget = SPIN_BUDGET;
  const int w4 = wave & 3;
  u32x8 w[16];
  {
    u32x4 s0[16], s1[16];
    wblk_load<256>(s0, p.Wv + h * 128, NH * 128, tid);
    wblk_load<1024>(s1, p.Wo + (size_t)h * 128 * DM, DM, tid);
    wblk_put<256>(l, s0, tid); bar();
    if (isv) {
      UNR for (int t = 0; t < 2; ++t) {
        const unsigned wb = wblk_base<256>(2 * w4 + t, lane);
        UNR for (int i = 0; i < 8; ++i) w[8 * t + i] = wblk_pair<256>(l, wb, i);
      }
    }
    bar(); wblk_put<1024>(l, s1, tid); bar();
    if (!isv) {
      UNR for (int ct = 0; ct < 8; ++ct) {
        const unsigned wb = wblk_base<1024>(8 * w4 + ct, lane);
        UNR for (int i = 0; i < 2; ++i) w[2 * ct + i] = wblk_pair<1024>(l, wb, i);
      }
    }
    bar();
  }
  TK_DECL
  const int c = lane & 15, g = lane >> 4;
  const unsigned l16 = opaque(lane * 16), a32 = opaque((g * 32 + c) * 16);
  const int sidx = (int)opaque(c < 8 ? 0x4444u : 0xEEEEu);
  const unsigned tw = opaque(timg_w(32 * w4 + c, g)), tr0 = opaque(timg_r(lane, 0)), tr1 = opaque(timg_r(lane, 1));
  const unsigned vmw = opaque(L_VM + (32 * w4 + 16 * (g >> 1) + c) * 16 + (g & 1) * 8);
  const unsigned vmr = opaque(L_VM + (16 * g + ((c >> 2) & 1) + 4 * (c >> 3) + (c & 2)) * 16 + (c & 1) * 8);
  const unsigned yvo = opaque(((g >> 1) + 2 * (g & 1)) * 8192 + c * 16);
  auto v_phase = [&](int sx) {
    const int b = sx & 1;
    f32x4 a0[2] = {zero4(), zero4()}, a1[2] = {zero4(), zero4()};
    x_gemm<true>(l, b, a32, w, a0, a1);
    TK(7);
    UNR for (int t = 0; t < 2; ++t) {
      const unsigned o = opaque((tw ^ (t * 32)) + b * 8192);
      lw64(l, L_VX + o, pk4(a0[t]));
      lw64(l, L_VX + o + 1024, pk4(a1[t]));
    }
    TK(8);
  };
  auto u_phase = [&](int sx) {
    const unsigned k0 = opaque(tr0 + (sx & 1) * 8192), k1 = opaque(tr1 + (sx & 1) * 8192);
    u32x4 vf[2][4];
    UNR for (int ks = 0; ks < 4; ++ks)
      UNR for (int t = 0; t < 2; ++t) vf[t][ks] = join2(lds_tr(l, L_VX + (k0 ^ (t * 32)) + ks * 2048), lds_tr(l, L_VX + (k1 ^ (t * 32)) + ks * 2048));
    SB();
    const unsigned ub = WS_U + (L * URING + (sx & (URING - 1))) * U_SLOT + h * 1024 + w4 * 8 * 32768;
    f32x4 a[2][2];
    UNR for (int ct = 0; ct <= 8; ++ct) {
      if (ct < 8) {
        UNR for (int ks = 0; ks < 4; ++ks) {
          const u32x4 b = half(w[2 * ct + (ks >> 1)], ks & 1);
          UNR for (int t = 0; t < 2; ++t) a[ct & 1][t] = mfma(vf[t][ks], b, ks ? a[ct & 1][t] : zero4());
        }
      }
      SB();
      if (ct) stg(r, l16, ub + (ct - 1) * 32768, join2(pk4(a[(ct - 1) & 1][0]), pk4(a[(ct - 1) & 1][1])));
      SB();
    }
  };
  auto u_flag = [&](int sx) {
    drain();
    if (lane == 0) __builtin_amdgcn_raw_buffer_store_b32((unsigned)sx, r, 0, flag_off(L, w4, h), 16);
  };
  xn_fetch(r, l, L, 0, -1, wave, l16, budget);
  xn_fetch(r, l, L, 1, -1, wave, l16, budget);
  drain();
  bar();
  if (isv) v_phase(0);
  bar();
  xn_fetch(r, l, L, 2, -1, wave, l16, budget);
  if (isv) { v_phase(1); drain(); } else { u_phase(0); u_flag(0); }
  bar();
  xn_fetch(r, l, L, 3, -1, wave, l16, budget);
  if (isv) v_phase(2); else u_phase(1);
  unsigned tref = 0, tb = 0, dur = 0;
  for (int s = 0; s < NSEG; ++s) {
    ST(s, 0);
    TK(11);
    const int xfv = flag_ld(r, flag_off(L, 4 + (s & 1), wave));
    if (isv) {
      if (V_PACE && s) wait_until(tref + V_PACE);
      u32x4 m[2];
      const unsigned mb = WS_MEM + (L * (NSEG + 1) + s) * MEM_SLOT + w4 * 2048;
      POLL(m, M_SLEEP, { m[0] = ldg(r, l16, mb); m[1] = ldg(r, l16 + 1024, mb); });
      lw128(l, L_MEM + w4 * 2048 + l16, m[0]); lw128(l, L_MEM + w4 * 2048 + 1024 + l16, m[1]);
    }
    TK(0);
    bar();
    TK(1);
    ST(s, 1);
    const int xf = __builtin_amdgcn_readfirstlane(xfv);
    f32x4 y[8];
    const unsigned yo = WS_Y + (L * NSEG + s) * Y_SLOT + w4 * 65536 + h * 256;
    if (isv) {
      if (V_PACE) tref = now();
      f32x4 a0 = zero4(), a1 = a0;
      u32x4 f[8];
      UNR for (int i = 0; i < 8; ++i) f[i] = lr128(l, L_MEM + l16 + i * 1024);
      SB();
      UNR for (int i = 0; i < 8; ++i) { a0 = smfmac(f[i], w[i], a0, sidx); a1 = smfmac(f[i], w[8 + i], a1, sidx); }
      lw64(l, vmw, pk4(fold32(a0, a1)));
    } else {
      if (BELL) tb = now();
      u_flag(s + 1);
      if (BELL && w4 == 0) bell(r, L, 7, h, tb + dur, s, lane);

      UNR for (int ct = 0; ct < 8; ++ct) { y[ct] = zero4(); asm volatile("" : "+v"(y[ct])); }
      __builtin_amdgcn_s_setprio(3);
    }
    TK(2);
    bar();
    TK(3);
    ST(s, 2);
    if (isv) {
      __builtin_amdgcn_s_sleep(V_NAP);
      if (!LATE_DMA && s + 4 < NSEG) xn_fetch(r, l, L, s + 4, xf, wave, l16, budget);
      TK(5);
      if (s + 3 < NSEG) v_phase(s + 3);
      if (LATE_DMA && s + 4 < NSEG) xn_fetch(r, l, L, s + 4, xf, wave, l16, budget);
      ST(s, 5);
    } else {
      u32x4 f[2];
      UNR for (int i = 0; i < 2; ++i) f[i] = join2(lds_tr(l, vmr + i * 1024), lds_tr(l, vmr + i * 1024 + 128));
      SB();
      UNR for (int ct = 0; ct < 4; ++ct) y[ct] = smfmac(f[0], w[2 * ct], y[ct], sidx);
      UNR for (int ct = 0; ct < 4; ++ct) y[ct] = smfmac(f[1], w[2 * ct + 1], y[ct], sidx);
      UNR for (int ct = 4; ct < 6; ++ct) y[ct] = smfmac(f[0], w[2 * ct], y[ct], sidx);
      SB();
      const f32x4 fa = fold32(y[0], y[1]);
      SB();
      UNR for (int ct = 6; ct < 8; ++ct) y[ct] = smfmac(f[0], w[2 * ct], y[ct], sidx);
      SB();
      const f32x4 fb = fold32(y[2], y[3]);
      SB();
      UNR for (int ct = 4; ct < 6; ++ct) y[ct] = smfmac(f[1], w[2 * ct + 1], y[ct], sidx);
      SB();
      const u32x2 pa = pk4(fa), pb = pk4(fb);
      SB();
      UNR for (int ct = 6; ct < 8; ++ct) y[ct] = smfmac(f[1], w[2 * ct + 1], y[ct], sidx);
      SB();
      stg(r, yvo, yo, unsent(pair16(pa, pb)));
      stg(r, yvo, yo + 32768, unsent(pair16(pk4(fold32(y[4], y[5])), pk4(fold32(y[6], y[7])))));
      if (BELL) dur = now() - tb;
      __builtin_amdgcn_s_setprio(1);
      STW(s, 3, 4);
      TK(4);
      if (!LATE_DMA && s + 4 < NSEG) xn_fetch(r, l, L, s + 4, xf, wave, l16, budget);
      if (U_NAP) { __builtin_amdgcn_s_setprio(0); __builtin_amdgcn_s_sleep(U_NAP); }
      if (s + 2 < NSEG) u_phase(s + 2);
      if (LATE_DMA && s + 4 < NSEG) xn_fetch(r, l, L, s + 4, xf, wave, l16, budget);
      STW(s, 4, 4);
      TK(7);
      STW(s, 6, 4);
      TK(8);
    }
  }
  TK_DUMP(32 + h);
}

DEV void role_R(const Params& p, rsrc_t r, lds_t l, int L, int rep, int rc, int tid, int wave, int lane) {
  constexpr unsigned L_RC = 0, L_RX = 8192;
  int budget = SPIN_BUDGET;
  const int c = lane & 15, g = lane >> 4;
  const unsigned l16 = opaque(lane * 16), pxo = opaque((c >> 3) * 640 + (g * 8 + (c & 7)) * 16), pmo = opaque(c < 8 ? g * 640 + 512 + c * 16 : 0x80000000u);
  const unsigned zo = opaque(g * 256 + (((c & 6) << 1) | ((c >> 3) << 1) | (c & 1)) * 16);
  const unsigned xwo = opaque(c * 16 + (g & 1) * 512 + (g >> 1) * 8);
  const unsigned xmo = opaque(g * 2560 + 1024 + c * 16), xvo = opaque(((g >> 1) * 32 + c) * 16);
  const unsigned mro = opaque((((lane >> 5) * 2 + ((lane >> 1) & 1)) * 16 + (wave & 1) * 8 + ((lane >> 2) & 7)) * 16 + (lane & 1) * 8);
  TK_DECL
  for (int s = rep; s < NSEG; s += 2) {
    ST(s, 0);
    for (;;) {
      const int f = (int)__builtin_amdgcn_raw_buffer_load_b32(r, (lane & 3) * 128, flag_off(L, rc >> 2, 4 * wave), 16);
      asm volatile("" ::: "memory");
      if (!any_lane(f < s) || --budget < 0) break;
      __builtin_amdgcn_s_sleep(4);
    }
    if (UGATE && s) {
      unsigned m1;
      for (;;) {
        m1 = __builtin_amdgcn_readfirstlane(__builtin_amdgcn_raw_buffer_load_b32(r, 0, flag_off(L, 6, rc), 16));
        asm volatile("" ::: "memory");
        if (((m1 - (unsigned)s + 1) & 63) < 2 || --budget < 0) break;
        __builtin_amdgcn_s_sleep(8);
      }
      if ((m1 & 63) != (unsigned)s)
        for (int i = 0; i < 4096 && (int)((now() << 6) - (m1 + (UGATE << 6))) < 0; ++i) {}
      asm volatile("" ::: "memory");
    }
    TK(0);
    u32x4 uf[4][2];
    const unsigned ub = WS_U + (L * URING + (s & (URING - 1))) * U_SLOT + 2 * rc * 32768 + wave * 4096;
    UNR for (int hh = 0; hh < 4; ++hh)
      UNR for (int ct = 0; ct < 2; ++ct)
        uf[hh][ct] = ldg(r, zo + hh * 1024, ub + ct * 32768);
    const unsigned xso = (L * NSEG + s) * X_SLOT + (2 * rc + (wave & 1)) * 1024 + ((wave >> 1) & 1) * 256;
    const u32x4 res = L ? ldg(r, xvo, WS_X + xso)
                        : *(const u32x4*)(p.x + (size_t)(32 * s + 16 * ((wave >> 1) & 1) + c) * DM + 32 * rc + 16 * (wave & 1) + 8 * (g >> 1));
    unsigned tref = 0;
    if (R_DELAY) {
      for (;;) {
        const unsigned m1 = __builtin_amdgcn_raw_buffer_load_b32(r, (lane & 15) * 512, WS_MEM + (L * (NSEG + 1) + s) * MEM_SLOT, 16);
        asm volatile("" ::: "memory");
        if (!any_lane(m1 == SENT) || --budget < 0) break;
      }
      tref = now();
      wait_until(tref + R_DELAY);
    }
    u32x4 q[5];
    const unsigned yb = WS_Y + (L * NSEG + s) * Y_SLOT + 2 * rc * 8192 + wave * 1024, pmb = WS_PM + (L * NSEG + s) * PM_SLOT + wave * 2560;
    if (BELL) {
      unsigned bv;
      for (;;) {
        bv = __builtin_amdgcn_raw_buffer_load_b32(r, (lane & 3) * 128 + (lane & 4) * 1024, flag_off(L, 6, 4 * wave), 16);
        asm volatile("" ::: "memory");
        if (!any_lane((bv & 63) != (unsigned)s) || --budget < 0) break;
      }
      bv += (lane & 4 ? Y_EXTRA : BELL) << 6;
      for (int i = 0; i < 4096 && any_lane((lane & 4) && (int)((now() << 6) - bv) < 0); ++i) {}
      asm volatile("" ::: "memory");
      q[3] = ldg(r, zo, yb); q[4] = ldg(r, zo, yb + 8192);
      asm volatile("" ::: "memory");
      for (int i = 0; i < 4096 && any_lane((int)((now() << 6) - bv) < 0); ++i) {}
      asm volatile("" ::: "memory");
      ST(s, 1);
      q[0] = ldg(r, pxo, pmb); q[1] = ldg(r, pxo + 1280, pmb); q[2] = ldg(r, pmo, pmb);
    }
    TK(1);
    if (!BELL || !all_there(q)) {
      POLL(q, 0, { TKC(10); q[3] = ldg(r, zo, yb); q[4] = ldg(r, zo, yb + 8192); q[0] = ldg(r, pxo, pmb); q[1] = ldg(r, pxo + 1280, pmb); q[2] = ldg(r, pmo, pmb); });
    }
    const u32x4 yf[2] = {q[3], q[4]};
    ST(s, 2);
    TK(2);
    {
      f32x4 lo[2], hi[2];
      UNR for (int ct = 0; ct < 2; ++ct) { lo[ct] = mfma(uf[0][ct], q[0], zero4()); hi[ct] = mfma(uf[1][ct], q[0], zero4()); }
      UNR for (int ct = 0; ct < 2; ++ct) { lo[ct] = mfma(uf[2][ct], q[1], lo[ct]); hi[ct] = mfma(uf[3][ct], q[1], hi[ct]); }
      lo[0] = mfma(yf[0], q[2], lo[0]); lo[1] = mfma(yf[1], q[2], lo[1]);
      f32x4 a;
      UNR for (int j = 0; j < 4; ++j) {
        const float own = c < 8 ? lo[0][j] : hi[1][j], oth = c < 8 ? lo[1][j] : hi[0][j];
        a[j] = own + ror8(oth);
      }
      lw128f(l, L_RC + wave * 1024 + l16, a);
    }
    TK(3);
    bar();
    TK(4);
    ST(s, 3);
    if (wave < 2) {
      __builtin_amdgcn_s_setprio(3);
      f32x2 t[8];
      UNR for (int w = 0; w < 8; ++w) t[w] = *(LAS f32x2*)(l + L_RC + mro + w * 1024);
      const f32x2 m = ((t[0] + t[1]) + (t[2] + t[3])) + ((t[4] + t[5]) + (t[6] + t[7]));
      __builtin_amdgcn_raw_buffer_store_b32(umin(pk(m[0], m[1]), SENT - 1), r, l16 >> 2, WS_MEM + (L * (NSEG + 1) + s + 1) * MEM_SLOT + rc * 512 + wave * 256, 16);
      __builtin_amdgcn_s_setprio(0);
    } else {
      __builtin_amdgcn_s_sleep(4);
    }
    ST(s, 4);
    TK(5);
    u32x4 px[10];
    {
      const unsigned b = WS_PX + (L * NSEG + s) * PX_SLOT + wave * 10240;
      POLL(px, 1, { UNR for (int i = 0; i < 8; ++i) px[i] = ldg(r, l16 + (i & 1) * 1280, b + (i >> 1) * 2560);
                    px[8] = ldg(r, xmo, b); px[9] = ldg(r, xmo + 1280, b); });
    }
    ST(s, 5);
    TK(6);
    UNR for (int t = 0; t < 2; ++t)
      UNR for (int ct = 0; ct < 2; ++ct) {
        f32x4 a = mfma(yf[ct], px[8 + t], zero4());
        UNR for (int hh = 0; hh < 4; ++hh) a = mfma(uf[hh][ct], px[2 * hh + t], a);
        lw64(l, L_RX + (wave * 4 + t * 2 + ct) * 1024 + xwo, (u32x2){f2u(a[0]), f2u(a[1])});
        lw64(l, L_RX + (wave * 4 + t * 2 + ct) * 1024 + xwo + 256, (u32x2){f2u(a[2]), f2u(a[3])});
      }
    TK(7);
    bar();
    TK(8);
    if (wave < 4) {
      const u32x2 m = pk4(sum8(l, L_RX + wave * 1024 + l16, 4096));
      const u32x4 v = unsent(add_bf8(res, pair16(m, m)));
      if (!(g & 1)) {
        stg(r, xvo, WS_X1 + xso, v);
        if (s + AHEAD < NSEG) stg(r, xvo, WS_X1 + xso + AHEAD * X_SLOT, sent4());
      }
    }
    if (s + AHEAD < NSEG) {
      const unsigned yb = WS_Y + (L * NSEG + s + AHEAD) * Y_SLOT + 2 * rc * 8192 + wave * 1024;
      stg(r, l16, yb, sent4()); stg(r, l16, yb + 8192, sent4());
    }
    ST(s, 6);
    TK(9);
  }
  TK_DUMP(64 + rep * 16 + rc);
}

DEV void role_F1(const Params& p, rsrc_t r, lds_t l, int L, int f, int tid, int wave, int lane) {
  int budget = SPIN_BUDGET;
  u32x8 w[16];
  load_w2(w, l, p.Wg + f * 128, p.Wu + f * 128, FF, tid, wave, lane);
  const f32x4 cg = colsum<false>(w, 0), cu = colsum<false>(w, 1);
  const unsigned l16 = opaque(lane * 16), a32 = opaque(((lane >> 4) * 32 + (lane & 15)) * 16);
  const unsigned fso = opaque(L_FS + wave * 256 + (lane & 15) * 4);
  u32x4 xv[4] = {sent4(), sent4(), sent4(), sent4()};
  for (int s = 0; s < NSEG; ++s) {
    const int b = s & 1;
    ST(s, 0);
    if (!all_there(xv)) { X_WAIT(xv, WS_X1 + (L * NSEG + s) * X_SLOT); }
    x_put(l, b, xv, wave, l16);
    bar();
    ST(s, 1);
    if (wave >= 4) x_final(l, b, wave, l16); else __builtin_amdgcn_s_setprio(1);
    f32x4 ag[2] = {zero4(), zero4()}, au[2] = {zero4(), zero4()};
    x_gemm<false>(l, b, a32, w, ag, au);
    X_ISSUE(xv, WS_X1 + (L * NSEG + (s + 1 < NSEG ? s + 1 : s)) * X_SLOT);
    if (wave < 4) { __builtin_amdgcn_s_setprio(0); x_final(l, b, wave, l16); }
    u32x2 hp[2];
    UNR for (int t = 0; t < 2; ++t) {
      const float rs = *(LAS float*)(l + fso + t * 64), mr = *(LAS float*)(l + fso + t * 64 + 128);
      const u32x2 gt = pk4(ag[t] * rs + cg * mr), ut = pk4(au[t] * rs + cu * mr);
      UNR for (int j = 0; j < 2; ++j) {
        const float g0 = bflo(gt[j]), g1 = bfhi(gt[j]);
        hp[t][j] = pk((g0 > 0.f ? g0 : g0 * 0.f) * bflo(ut[j]), (g1 > 0.f ? g1 : g1 * 0.f) * bfhi(ut[j]));
      }
    }
    const unsigned o = WS_H + (L * NSEG + s) * H_SLOT + (f * 16 + 2 * wave) * 512;
    stg(r, l16, o, unsent(pair16(hp[0], hp[1])));
    if (s + AHEAD < NSEG) stg(r, l16, o + AHEAD * H_SLOT, sent4());
    STW(s, 2, 7);
  }
}

DEV void role_F2(const Params& p, rsrc_t r, lds_t l, int L, int rep, int cs, int tid, int wave, int lane) {
  int budget = SPIN_BUDGET;
  const int c = lane & 15, g = lane >> 4;
  u32x4 wd[7][4];
  {
    u32x4 st[28];
    const __hip_bfloat16* base = p.Wd + (size_t)(224 * wave) * DM + 64 * cs;
    UNR for (int i = 0; i < 28; ++i) st[i] = *(const u32x4*)(base + (size_t)(8 * i + (lane >> 3)) * DM + (lane & 7) * 8);
    lds_t lw = l + wave * 16384;
    const unsigned q = (lane & 15) >> 2, pp = lane & 3;
    UNR for (int half = 0; half < 2; ++half) {
      const int n = half ? 12 : 16;
      UNR for (int i = 0; i < 16; ++i) if (i < n) {
        const unsigned row = 8 * i + (lane >> 3), sw = ((row >> 1) & 1) | ((row >> 2) & 2);
        lw128(lw, row * 128 + (((lane & 7) ^ (sw << 1)) << 4), st[half * 16 + i]);
      }
      bar();
      UNR for (int k = 0; k < 4; ++k) if (half * 4 + k < 7) {
        UNR for (int ct = 0; ct < 4; ++ct) {
          const unsigned r0 = 32 * k + 8 * g + q, r1 = r0 + 4;
          const unsigned s0 = ((r0 >> 1) & 1) | ((r0 >> 2) & 2), s1 = ((r1 >> 1) & 1) | ((r1 >> 2) & 2);
          const u32x2 a = lds_tr(lw, r0 * 128 + ((ct ^ s0) << 5) + pp * 8), b = lds_tr(lw, r1 * 128 + ((ct ^ s1) << 5) + pp * 8);
          wd[half * 4 + k][ct] = (u32x4){a.x, a.y, b.x, b.y};
        }
      }
      bar();
    }
  }
  const unsigned l16 = opaque(lane * 16), a32 = opaque((g * 32 + c) * 16), xvo = opaque(((g >> 1) * 32 + c) * 16);
  for (int s = rep; s < NSEG; s += 2) {
    const unsigned hb = WS_H + (L * NSEG + s) * H_SLOT + wave * 28 * 512;
    ST(s, 0);
    { u32x4 h1[1]; POLL(h1, 4, h1[0] = ldg(r, a32, hb + 6 * 2048)); }
    ST(s, 1);
    u32x4 hf[14];
    POLL(hf, 1, { UNR for (int i = 0; i < 14; ++i) hf[i] = ldg(r, a32 + (i & 1) * 256, hb + (i >> 1) * 2048); });
    ST(s, 2);
    const unsigned xso = (L * NSEG + s) * X_SLOT + (8 * cs + 2 * (wave & 3)) * 512 + (wave >> 2) * 256;
    const u32x4 res = ldg(r, xvo, WS_X1 + xso);
    f32x4 a[2][4];
    UNR for (int t = 0; t < 2; ++t)
      UNR for (int ct = 0; ct < 4; ++ct) a[t][ct] = zero4();
    UNR for (int k = 0; k < 7; ++k)
      UNR for (int t = 0; t < 2; ++t)
        UNR for (int ct = 0; ct < 4; ++ct) a[t][ct] = mfma(wd[k][ct], hf[2 * k + t], a[t][ct]);
    const unsigned rb = ((s >> 1) & 1) * 65536;
    const unsigned wo_ = opaque(l16 + rb + wave * 8192), ro_ = opaque(l16 + rb + wave * 1024);
    UNR for (int t = 0; t < 2; ++t)
      UNR for (int ct = 0; ct < 4; ++ct) lw128f(l, wo_ + (t * 4 + ct) * 1024, a[t][ct]);
    bar();
    const u32x2 m = pk4(sum8(l, ro_, 8192));
    const u32x4 v = add_bf8(res, pair16(m, m));
    if (!(g & 1)) {
      if (L == 0) {
        stg(r, xvo, WS_X + NSEG * X_SLOT + xso, unsent(v));
        if (s + AHEAD < NSEG) stg(r, xvo, WS_X + NSEG * X_SLOT + xso + AHEAD * X_SLOT, sent4());
      } else {
        *(u32x4*)(p.out + (size_t)(32 * s + 16 * (wave >> 2) + c) * DM + 64 * cs + 16 * (wave & 3) + 8 * (g >> 1)) = v;
      }
    }
    ST(s, 3);
  }
}

DEV void role_N(const Params& p, rsrc_t r, lds_t l, int L, int par, int tid, int wave, int lane) {
  int budget = SPIN_BUDGET;
  const unsigned l16 = opaque(lane * 16);
  for (int s = par; s < NSEG; s += 2) {
    const int b = (s >> 1) & 1;
    u32x4 xv[4];
    if (L) { X_WAIT(xv, WS_X + (NSEG + s) * X_SLOT); }
    else { UNR for (int i = 0; i < 4; ++i) xv[i] = *(const u32x4*)(p.x + (size_t)(32 * s + (lane & 31)) * DM + 64 * wave + 16 * i + 8 * (lane >> 5)); }
    x_part(l, b, xv, wave, l16);
    bar();
    float mean, rstd;
    x_rowstats(l, b, l16, mean, rstd);
    UNR for (int i = 0; i < 4; ++i) {
      u32x4 o;
      UNR for (int j = 0; j < 4; ++j) o[j] = pk((bflo(xv[i][j]) - mean) * rstd, (bfhi(xv[i][j]) - mean) * rstd);
      stg(r, l16 + i * 1024, WS_XN + (L * NSEG + s) * X_SLOT + wave * 4096, o);
    }
    drain();
    if (lane == 0) __builtin_amdgcn_raw_buffer_store_b32((unsigned)s, r, 0, flag_off(L, 4 + par, wave), 16);
  }
}

__global__ void __launch_bounds__(512, 2) rmt_mega(Params p) {
  extern __shared__ __attribute__((aligned(16))) unsigned char smem[];
  lds_t l = (lds_t)smem;
  const int tid = __builtin_amdgcn_workitem_id_x(), lane = tid & 63, wave = __builtin_amdgcn_readfirstlane(tid >> 6);
  const rsrc_t r = __builtin_amdgcn_make_buffer_rsrc(p.ws, 0, RS_END, 0x00020000);
  const int b = __builtin_amdgcn_workgroup_id_x(), L = (b >> 2) & 1;
  const int xq = b & 3, j = b >> 3;
  const int idx = MAP == 0 ? xq + 4 * j
                : MAP == 1 ? xq * 32 + j
                : MAP == 2 ? ((xq >> 1) * 32 + (j >> 4) * 64 + (xq & 1) + 2 * (j & 15))
                : MAP == 5 ? (xq < 2 ? xq * 32 + j : (j < 16 ? 64 : 80) + (xq - 2) * 16 + j)
                : MAP == 6 ? (xq < 2 ? xq * 16 + (j & 15) + (j >> 4) * 32 : (j < 16 ? 64 : 80) + (xq - 2) * 16 + j)
                : MAP == 7 ? (xq == 0 ? j : xq == 1 ? 96 + j : (j < 16 ? 64 : 16) + (xq - 2) * 16 + j)
                : MAP == 8 ? (xq < 2 ? xq * 32 + j : (j < 16 ? 64 + (xq - 2) + 2 * j : 80 + (xq - 2) * 16 + j))
                : MAP == 9 ? (xq < 2 ? xq * 16 + (j & 15) + (j >> 4) * 32 : (j < 16 ? 64 + (xq - 2) + 2 * j : 80 + (xq - 2) * 16 + j))
                : MAP == 4 ? (xq == 0 ? j : xq == 1 ? 64 + j : (j < 16 ? 32 : 80) + (xq - 2) + 2 * (j & 15) + (j >> 4) * 16)
                           : (xq == 0 ? j : (j < 10 ? 32 + (xq - 1) + 3 * j : j < 21 ? 62 + (xq - 1) + 3 * (j - 10) : 95 + (xq - 1) + 3 * (j - 21)));
  if (idx < 32) {
    u32x8 w[16];
    load_w2(w, l, p.Wq + idx * 128, p.Wk + idx * 128, NH * 128, tid, wave, lane);
    if (wave == 0) role_S<0>(w, r, l, L, idx, tid, wave, lane);
    else if (wave < 3) role_S<1>(w, r, l, L, idx, tid, wave, lane);
    else role_S<2>(w, r, l, L, idx, tid, wave, lane);
  }
  else if (idx < 64) { if (wave < 4) role_V<true>(p, r, l, L, idx - 32, tid, wave, lane); else role_V<false>(p, r, l, L, idx - 32, tid, wave, lane); }
  else if (idx < 96) role_R(p, r, l, L, (idx - 64) >> 4, (idx - 64) & 15, tid, wave, lane);
  else if (idx < 110) role_F1(p, r, l, L, idx - 96, tid, wave, lane);
  else if (idx < 126) role_F2(p, r, l, L, (idx - 110) >> 3, (idx - 110) & 7, tid, wave, lane);
  else role_N(p, r, l, L, idx - 126, tid, wave, lane);
}

constexpr unsigned INIT_BLOCKS = 512;
__global__ void __launch_bounds__(256) rmt_init(const __hip_bfloat16* mem0, unsigned char* ws) {
  const unsigned t = __builtin_amdgcn_workgroup_id_x() * 256 + __builtin_amdgcn_workitem_id_x(), nt = INIT_BLOCKS * 256;
  for (unsigned i = t; i < WS_SMALL_END / 16; i += nt) {
    u32x4 v = sent4();
    const unsigned o = i * 16, o1 = o - (NSEG + 1) * MEM_SLOT;
    if (o < MEM_SLOT || o1 < MEM_SLOT) { const unsigned n = (o < MEM_SLOT ? o : o1) >> 4;
      const unsigned* src = (const unsigned*)(mem0 + (n & 7) * DM + (n >> 4) * 16) + ((n >> 3) & 1);
      v = unsent((u32x4){src[0], src[2], src[4], src[6]});
    }
    *(u32x4*)(ws + o) = v;
  }
  auto arm = [&](unsigned base, unsigned bytes) { for (unsigned i = t; i < bytes / 16; i += nt) *(u32x4*)(ws + base + i * 16) = sent4(); };
  arm(WS_X + NSEG * X_SLOT, AHEAD * X_SLOT);
  for (int L = 0; L < 2; ++L) {
    arm(WS_X1 + L * NSEG * X_SLOT, AHEAD * X_SLOT);
    arm(WS_PX + L * NSEG * PX_SLOT, AHEAD * PX_SLOT);
    arm(WS_H + L * NSEG * H_SLOT, AHEAD * H_SLOT);
    arm(WS_Y + L * NSEG * Y_SLOT, AHEAD * Y_SLOT);
  }
}

#ifdef NOPK_PUSHED
#pragma clang attribute pop
#endif

extern "C" void kernel_launch(void* const* d_in, const int* in_sizes, int n_in, void* d_out, int out_size, void* d_ws, size_t ws_size,
                              hipStream_t stream) {
  Params p;
  p.x = (const __hip_bfloat16*)d_in[0];
  p.Wq = (const __hip_bfloat16*)d_in[2]; p.Wk = (const __hip_bfloat16*)d_in[3]; p.Wv = (const __hip_bfloat16*)d_in[4];
  p.Wo = (const __hip_bfloat16*)d_in[5]; p.Wg = (const __hip_bfloat16*)d_in[8]; p.Wu = (const __hip_bfloat16*)d_in[9];
  p.Wd = (const __hip_bfloat16*)d_in[10];
  p.out = (__hip_bfloat16*)d_out; p.ws = (unsigned char*)d_ws;
  (void)hipFuncSetAttribute((const void*)rmt_mega, hipFuncAttributeMaxDynamicSharedMemorySize, LDS_BYTES);
  rmt_init<<<INIT_BLOCKS, 256, 0, stream>>>((const __hip_bfloat16*)d_in[1], (unsigned char*)d_ws);
  rmt_mega<<<256, 512, LDS_BYTES, stream>>>(p);
}
```

```cpp
#include <hip/hip_runtime.h>
#include <hip/hip_bf16.h>
#if defined(__HIP_DEVICE_COMPILE__) && __HIP_DEVICE_COMPILE__
#pragma clang attribute push(__attribute__((target("no-packed-fp32-ops"))), apply_to = function)
#define NOPK_PUSHED 1
#endif

typedef __attribute__((ext_vector_type(8))) __bf16 bf16x8;
typedef __attribute__((ext_vector_type(16))) __bf16 bf16x16;
typedef __attribute__((ext_vector_type(2))) __bf16 bf16x2;
typedef __attribute__((ext_vector_type(8))) unsigned u32x8;
typedef __attribute__((ext_vector_type(4))) float f32x4;
typedef __attribute__((ext_vector_type(2))) float f32x2;
typedef __attribute__((ext_vector_type(4))) unsigned u32x4;
typedef __attribute__((ext_vector_type(2))) unsigned u32x2;
typedef __attribute__((ext_vector_type(4))) short s16x4;
#define LAS __attribute__((address_space(3)))
#define DEV __device__ __forceinline__
typedef __amdgpu_buffer_rsrc_t rsrc_t;
typedef LAS unsigned char* lds_t;

constexpr int NSEG = 64, DM = 512, NH = 32, FF = 1792;
constexpr int AHEAD = 4;
constexpr unsigned BLK = 8192, X_SLOT = 32768, PX_SLOT = 65536, H_SLOT = 114688, MB = 1u << 20;
constexpr unsigned V_MEM = 0;
constexpr unsigned V_PM = 32 * BLK;
constexpr unsigned V_FL = 64 * BLK;
constexpr unsigned V_Y = 1 * MB;
constexpr unsigned V_X1 = 2 * MB, V_X2 = 4 * MB, V_XN = 6 * MB, V_H = 8 * MB;
constexpr unsigned V_U = 15 * MB;
constexpr unsigned V_PX = 19 * MB;
constexpr int NREG = 20;
constexpr unsigned WS_CAL = 120 * 2 * BLK, WS_END = NREG * 2 * MB;
enum { FL_U = 0, FL_XN = 4, FL_S = 6, FL_V = 7, FL_RX = 8, FL_F1 = 10, FL_F2 = 11, FL_ARM = 12 };
constexpr unsigned RS_END = WS_END;
constexpr int LDS_BYTES = 160 * 1024;
constexpr unsigned SENT = 0xFFFFFFFFu, OOB = 0x80000000u;
constexpr int SPIN_BUDGET = 1 << 20;
#define MAP 9
#define BELL 4
#define Y_EXTRA 12
#define XBELL 11
#define U_NAP 1
#define S_PRE 2
#define U_PRIO 1
#define UGATE 60
#define V_DMA 1
#define NCOPY 2
#define UEARLY 0
#define POLL_PRIO 0
#define PX_NAP 9
#define ABL_V 0
#define ABL_S 0
#define CALM 2
#define GUESS 0
#define XPERM 0x3210
#define XPERM1 XPERM
#define S_NAP2 4
#define DMA4 5
#define S_NAP 4
#define V_NAP 7
#define CAL_N 3
#define DLY_S 200
#define DLY_O 500
#define NEAR_FLIP 0u

struct Params {
  const __hip_bfloat16 *x, *Wq, *Wk, *Wv, *Wo, *Wg, *Wu, *Wd;
  __hip_bfloat16* out;
  unsigned char* ws;
};

#define MINI 0
#define ST(s, i)
#define STW(s, i, w)
#define STM(s, i, w)
#define TK_DECL
#define TK(i)
#define TKC(i)
#define TK_DUMP(id)
DEV unsigned f2u(float x) { return __builtin_bit_cast(unsigned, x); }
DEV float u2f(unsigned x) { return __builtin_bit_cast(float, x); }
DEV bool any_lane(bool x) { return __builtin_amdgcn_ballot_w64(x) != 0; }
DEV unsigned umin(unsigned a, unsigned b) { return a < b ? a : b; }
DEV unsigned umax(unsigned a, unsigned b) { return a > b ? a : b; }
DEV float fmaxn(float a, float b) { return __builtin_elementwise_maximum(a, b); }
DEV unsigned opaque(unsigned x) { asm volatile("" : "+v"(x)); return x; }
DEV f32x4 zero4() { return (f32x4){0.f, 0.f, 0.f, 0.f}; }
DEV u32x4 sent4() { return (u32x4){SENT, SENT, SENT, SENT}; }
DEV f32x4 mfma(u32x4 a, u32x4 b, f32x4 c) {
  return __builtin_amdgcn_mfma_f32_16x16x32_bf16(__builtin_bit_cast(bf16x8, a), __builtin_bit_cast(bf16x8, b), c, 0, 0, 0);
}
DEV f32x4 mfma16(u32x2 a, u32x2 b, f32x4 c) {
  return __builtin_amdgcn_mfma_f32_16x16x16bf16_1k(__builtin_bit_cast(s16x4, a), __builtin_bit_cast(s16x4, b), c, 0, 0, 0);
}
DEV f32x4 smfmac(u32x4 a, u32x8 b, f32x4 c, int idx) {
  return __builtin_amdgcn_smfmac_f32_16x16x64_bf16(__builtin_bit_cast(bf16x8, a), __builtin_bit_cast(bf16x16, b), c, idx, 0, 0);
}
DEV u32x4 half(const u32x8& v, int h) { return h ? __builtin_shufflevector(v, v, 4, 5, 6, 7) : __builtin_shufflevector(v, v, 0, 1, 2, 3); }
DEV u32x8 join(u32x4 a, u32x4 b) { return __builtin_shufflevector(a, b, 0, 1, 2, 3, 4, 5, 6, 7); }
DEV u32x4 join2(u32x2 a, u32x2 b) { return __builtin_shufflevector(a, b, 0, 1, 2, 3); }
DEV f32x4 fold32(f32x4 a, f32x4 b) {
  f32x4 o;
  _Pragma("unroll") for (int j = 0; j < 4; ++j) {
    auto t = __builtin_amdgcn_permlane32_swap(f2u(a[j]), f2u(b[j]), false, false);
    o[j] = u2f(t[0]) + u2f(t[1]);
  }
  return o;
}
DEV float ror8(float x) { return u2f(__builtin_amdgcn_update_dpp(0, f2u(x), 0x128, 0xf, 0xf, true)); }
DEV float dppf(float x, int i) {
  const int v = (int)f2u(x);
  return u2f(i == 0 ? __builtin_amdgcn_update_dpp(0, v, 0xB1, 0xf, 0xf, true) : i == 1 ? __builtin_amdgcn_update_dpp(0, v, 0x4E, 0xf, 0xf, true)
           : i == 2 ? __builtin_amdgcn_update_dpp(0, v, 0x124, 0xf, 0xf, true) : __builtin_amdgcn_update_dpp(0, v, 0x128, 0xf, 0xf, true));
}
DEV unsigned pk(float lo, float hi) { bf16x2 v = {(__bf16)lo, (__bf16)hi}; return __builtin_bit_cast(unsigned, v); }
DEV float bflo(unsigned u) { return u2f(u << 16); }
DEV float bfhi(unsigned u) { return u2f(u & 0xffff0000u); }
DEV float dot2(unsigned a, unsigned b, float c) {
  return __builtin_amdgcn_fdot2_f32_bf16(__builtin_bit_cast(bf16x2, a), __builtin_bit_cast(bf16x2, b), c, false);
}
DEV u32x4 ldg(rsrc_t r, unsigned voff, unsigned soff) { return __builtin_amdgcn_raw_buffer_load_b128(r, voff, soff, 16); }
DEV void stg(rsrc_t r, unsigned voff, unsigned soff, u32x4 v) {
  __builtin_amdgcn_raw_buffer_store_b128(v, r, voff, soff, 16);
  asm volatile("s_nop 1" ::"v"(v) : "memory");
}
DEV u32x4 unsent(u32x4 v) {
  v.x = umin(v.x, SENT - 1); v.y = umin(v.y, SENT - 1); v.z = umin(v.z, SENT - 1); v.w = umin(v.w, SENT - 1); return v;
}
DEV u32x4 lr128(lds_t l, unsigned off) { return *(LAS u32x4*)(l + off); }
DEV f32x4 lr128f(lds_t l, unsigned off) { return *(LAS f32x4*)(l + off); }
DEV void lw128(lds_t l, unsigned off, u32x4 v) { *(LAS u32x4*)(l + off) = v; }
DEV void lw128f(lds_t l, unsigned off, f32x4 v) { *(LAS f32x4*)(l + off) = v; }
DEV void lw64(lds_t l, unsigned off, u32x2 v) { *(LAS u32x2*)(l + off) = v; }
DEV u32x2 lds_tr(lds_t l, unsigned off) {
  return __builtin_bit_cast(u32x2, __builtin_amdgcn_ds_read_tr16_b64_v4i16((LAS s16x4*)(l + off)));
}
DEV unsigned now() { return (unsigned)__builtin_amdgcn_s_memrealtime(); }
DEV unsigned since(unsigned t, unsigned cap) { const unsigned d = (now() - t) & 0x1ffffffu; return d > cap ? 0 : d; }
DEV void nap_us(int ticks) { const unsigned t = now(); while (now() - t < (unsigned)ticks) __builtin_amdgcn_s_sleep(8); }
DEV void drain() { asm volatile("s_waitcnt vmcnt(0)" ::: "memory"); }
DEV void bar() {
  __builtin_amdgcn_fence(__ATOMIC_RELEASE, "workgroup", "local");
  __builtin_amdgcn_s_barrier();
  __builtin_amdgcn_fence(__ATOMIC_ACQUIRE, "workgroup", "local");
}
#define SB() __builtin_amdgcn_sched_barrier(0)
#define unlikely(x) __builtin_expect(!!(x), 0)
#define likely(x) __builtin_expect(!!(x), 1)
DEV unsigned pin_s(unsigned x) { x = __builtin_amdgcn_readfirstlane(x); asm volatile("" : "+s"(x)); return x; }
#define UNR _Pragma("unroll")
DEV float xmax(float x) {
  auto s = __builtin_amdgcn_permlane16_swap(f2u(x), f2u(x), false, false);
  x = fmaxn(u2f(s[0]), u2f(s[1]));
  auto t = __builtin_amdgcn_permlane32_swap(f2u(x), f2u(x), false, false);
  return fmaxn(u2f(t[0]), u2f(t[1]));
}
DEV float xsum(float x) {
  auto s = __builtin_amdgcn_permlane16_swap(f2u(x), f2u(x), false, false);
  x = u2f(s[0]) + u2f(s[1]);
  auto t = __builtin_amdgcn_permlane32_swap(f2u(x), f2u(x), false, false);
  return u2f(t[0]) + u2f(t[1]);
}
DEV u32x4 pair16(u32x2 a, u32x2 b) {
  auto r0 = __builtin_amdgcn_permlane16_swap(a.x, b.x, false, false);
  auto r1 = __builtin_amdgcn_permlane16_swap(a.y, b.y, false, false);
  u32x4 v = {r0[0], r1[0], r0[1], r1[1]};
  return v;
}
DEV u32x2 pk4(f32x4 a) { u32x2 v = {pk(a[0], a[1]), pk(a[2], a[3])}; return v; }
DEV u32x4 add_bf8(u32x4 a, u32x4 b) {
  u32x4 r;
  UNR for (int i = 0; i < 4; ++i) r[i] = pk(bflo(a[i]) + bflo(b[i]), bfhi(a[i]) + bfhi(b[i]));
  return r;
}
DEV f32x4 sum8(lds_t l, unsigned off, unsigned stride) {
  f32x4 t[8];
  UNR for (int w = 0; w < 8; ++w) t[w] = lr128f(l, off + w * stride);
  return ((t[0] + t[1]) + (t[2] + t[3])) + ((t[4] + t[5]) + (t[6] + t[7]));
}
template <int N>
DEV bool all_there(const u32x4 (&v)[N]) {
  unsigned m = 0;
  UNR for (int i = 0; i < N; ++i) m = umax(umax(umax(umax(m, v[i].x), v[i].y), v[i].z), v[i].w);
  return !any_lane(m == SENT);
}
#define POLL(v, SLEEP, ISSUE)                                   \
  for (;;) {                                                    \
    ISSUE;                                                      \
    const bool ok_ = all_there(v);                              \
    asm volatile("" ::: "memory");                              \
    if (ok_ || --budget < 0) break;                             \
    if (SLEEP) __builtin_amdgcn_s_sleep(SLEEP);                 \
  }

DEV unsigned swz8(unsigned row) { return (row & 3) | ((row >> 1) & 4); }
template <int ROWB>
DEV void wblk_load(u32x4 (&st)[16], const __hip_bfloat16* base, unsigned ld, int tid) {
  constexpr unsigned CPR = ROWB / 16;
  UNR for (int i = 0; i < 16; ++i) {
    const unsigned n = i * 512 + tid, row = n / CPR, ch = n % CPR;
    st[i] = *(const u32x4*)(base + (size_t)row * ld + ch * 8);
  }
}
template <int ROWB>
DEV void wblk_put(lds_t l, const u32x4 (&st)[16], int tid) {
  constexpr unsigned CPR = ROWB / 16;
  unsigned base[2];
  UNR for (int i = 0; i < 2; ++i) {
    const unsigned n = i * 512 + tid, row = n / CPR, ch = n % CPR;
    base[i] = opaque(row * ROWB + ((ch ^ (swz8(row) << 1)) << 4));
  }
  UNR for (int i = 0; i < 8; ++i) lw128(l, base[i & 1] + (i >> 1) * 16384, st[i]);
  UNR for (int i = 0; i < 2; ++i) base[i] = opaque(base[i] + 65536);
  UNR for (int i = 8; i < 16; ++i) lw128(l, base[i & 1] + ((i - 8) >> 1) * 16384, st[i]);
}
template <int ROWB>
DEV unsigned wblk_base(unsigned c16, int lane) {
  const unsigned r0 = 8 * (lane >> 4) + ((lane & 15) >> 2);
  return opaque(r0 * ROWB + ((c16 ^ swz8(r0)) << 5) + (lane & 3) * 8);
}
template <int ROWB>
DEV u32x8 wblk_pair(lds_t l, unsigned base, int kp) {
  constexpr unsigned KP = 64 * ROWB;
  const unsigned hi = (kp * KP) & ~65535u, o = kp * KP - hi;
  const unsigned b = hi ? base + hi : base;
  return join(join2(lds_tr(l, b + o), lds_tr(l, b + o + 4 * ROWB)), join2(lds_tr(l, b + o + 32 * ROWB), lds_tr(l, b + o + 36 * ROWB)));
}
DEV void load_w2(u32x8 (&w)[16], lds_t l, const __hip_bfloat16* w0, const __hip_bfloat16* w1, unsigned ld, int tid, int wave, int lane) {
  u32x4 s0[16], s1[16];
  wblk_load<256>(s0, w0, ld, tid);
  wblk_load<256>(s1, w1, ld, tid);
  wblk_put<256>(l, s0, tid); bar();
  const unsigned wb = wblk_base<256>(wave, lane);
  UNR for (int i = 0; i < 8; ++i) w[i] = wblk_pair<256>(l, wb, i);
  bar(); wblk_put<256>(l, s1, tid); bar();
  UNR for (int i = 0; i < 8; ++i) w[8 + i] = wblk_pair<256>(l, wb, i);
  bar();
}
DEV u32x4 ones4() { return (u32x4){0x3F803F80u, 0x3F803F80u, 0x3F803F80u, 0x3F803F80u}; }

DEV unsigned ph(unsigned nr, unsigned v) { return ((v & ~(BLK - 1)) << 1) | (v & (BLK - 1)) | (((nr >> (v >> 20)) & 1) << 13); }
DEV unsigned flag_off(unsigned nr, int row, int i) { return ph(nr, V_FL + row * 4096 + i * 128); }
DEV void flag_st(rsrc_t r, unsigned off, unsigned v, int lane) { __builtin_amdgcn_raw_buffer_store_b32(v, r, lane ? OOB : 0u, off, 16); }
DEV unsigned ld32(rsrc_t r, unsigned voff, unsigned soff) { return __builtin_amdgcn_raw_buffer_load_b32(r, voff, soff, 16); }
DEV void bell(rsrc_t r, unsigned nr, int row, int i, unsigned t, int s, int lane) { flag_st(r, flag_off(nr, row, i), (t << 7) | (unsigned)s, lane); }
DEV bool bell_rung(unsigned bv, int s) { return !any_lane((bv & 127) - (unsigned)s > 63u - (unsigned)s); }
template <int SLEEP>
DEV unsigned bell_get(rsrc_t r, unsigned voff, unsigned soff, unsigned bv, int s, int& budget) {
  while (!bell_rung(bv, s) && --budget >= 0) {
    if (SLEEP) __builtin_amdgcn_s_sleep(SLEEP);
    bv = ld32(r, voff, soff);
    asm volatile("" ::: "memory");
  }
  return bv;
}
DEV void bell_wait(unsigned bv, int s, int margin) {
  unsigned long long rt, ck;
  asm volatile("s_memrealtime %0\n\ts_memtime %1\n\ts_waitcnt lgkmcnt(0)" : "=&s"(rt), "=&s"(ck)::"memory");
  const unsigned d = (bv + (margin << 7) - ((unsigned)rt << 7)) >> 7;
  const unsigned due = (unsigned)ck + ((bv & 127) == (unsigned)s && d < 400u ? d * 22 : 0u);
  if (CALM) while (any_lane((int)(due - (unsigned)__builtin_amdgcn_s_memtime()) > 200)) __builtin_amdgcn_s_sleep(2);
  while (any_lane((int)(due - (unsigned)__builtin_amdgcn_s_memtime()) > 0)) {}
  asm volatile("" ::: "memory");
}

DEV void armed(rsrc_t r, unsigned nr, int lane, int& budget) {
  for (;;) {
    const unsigned a = ld32(r, (lane & 15) * 128 + (lane & 16) * 256, flag_off(nr, FL_ARM, 0));
    asm volatile("" ::: "memory");
    if (!any_lane(a == SENT) || --budget < 0) break;
    __builtin_amdgcn_s_sleep(2);
  }
}

constexpr unsigned L_MEM = 0, L_ST = 8192, L_FS = 12288, L_M0 = 14336, L_T = 18432, L_W0 = 65536, L_XS = 98304;
DEV void x_put(lds_t l, int buf, const u32x4 (&v)[4], int wave, unsigned l16) {
  const unsigned wb = opaque(l16 + (L_XS + (buf ^ 1) * 32768 + wave * 4096));
  UNR for (int i = 0; i < 4; ++i) lw128(l, wb + i * 1024, v[i]);
  float s1 = 0.f, s2 = 0.f;
  UNR for (int i = 0; i < 4; ++i)
    UNR for (int j = 0; j < 4; ++j) { s1 = dot2(v[i][j], 0x3F803F80u, s1); s2 = dot2(v[i][j], v[i][j], s2); }
  asm volatile("s_nop 3" : "+v"(s1), "+v"(s2));
  auto a = __builtin_amdgcn_permlane32_swap(f2u(s1), f2u(s2), false, false);
  const float t = u2f(a[0]) + u2f(a[1]);
  *(LAS float*)(l + (L_ST + buf * 2048 + (wave >> 1) * 512 + (wave & 1) * 8) + ((l16 & 0x1f0) + ((l16 >> 9) << 2))) = t;
}
DEV void x_final(lds_t l, int buf, int wave, unsigned l16) {
  const unsigned o = opaque((l16 & 0x1f0) + (l16 >> 9) * 1024 + buf * 2048);
  const f32x4 a = lr128f(l, L_ST + o) + lr128f(l, L_ST + o + 512);
  auto t = __builtin_amdgcn_permlane32_swap(f2u(a[0] + a[2]), f2u(a[1] + a[3]), false, false);
  const float u = u2f(t[0]) + u2f(t[1]);
  auto z = __builtin_amdgcn_permlane32_swap(f2u(u), f2u(u), false, false);
  const float mean = u2f(z[0]) * (1.f / DM);
  const float rstd = __builtin_amdgcn_rsqf(__builtin_fmaxf(u2f(z[1]) * (1.f / DM) - mean * mean, 0.f) + 1e-5f);
  *(LAS float*)(l + L_FS + wave * 256 + (l16 >> 2)) = l16 < 512 ? rstd : -mean * rstd;
}
DEV void x_pre(lds_t l, int buf, unsigned a32, u32x4 (&f)[3][4], bool extra = false, unsigned wx = 0) {
  const unsigned xb = opaque(a32 + (L_XS + (buf ^ 1) * 32768));
  UNR for (int i = 0; i < 8; ++i) {
    f[i >> 2][i & 3] = lr128(l, xb + (i >> 1) * 2048 + (i & 1) * 256);
    if (extra && (i & 1)) f[2][i >> 1] = lr128(l, wx + (i >> 1) * 1024);
  }
  SB();
}
template <bool WB, int XT, int ABL = 0, class Hook>
DEV void x_gemm(lds_t l, int buf, unsigned a32, const u32x8 (&w)[16], f32x4 (&a0)[2], f32x4 (&a1)[2], u32x4 (&f)[3][4], Hook hook,
                unsigned wx, f32x4& ax) {
  const unsigned xb = opaque(a32 + (L_XS + (buf ^ 1) * 32768));
  UNR for (int gr = 0; gr < 8; ++gr) {
    hook(gr);
    SB();
    UNR for (int i = 0; i < 4; ++i) {
      const u32x4 x = f[gr & 1][i], w0 = half(w[gr], i >> 1), w1 = half(w[8 + gr], i >> 1);
      if (!(ABL & 1) || gr == 0) a0[i & 1] = WB ? mfma(x, w0, a0[i & 1]) : mfma(w0, x, a0[i & 1]);
      if (!(ABL & 2) || gr == 0) a1[i & 1] = WB ? mfma(x, w1, a1[i & 1]) : mfma(w1, x, a1[i & 1]);
      if ((i & 1) == XT) ax = mfma(x, f[2][(gr & 1) * 2 + (i >> 1)], ax);
      SB();
      if (gr + 2 < 8) {
        f[gr & 1][i] = lr128(l, xb + ((gr + 2) * 2 + (i >> 1)) * 2048 + (i & 1) * 256);
        if ((i & 1) == XT) f[2][(gr & 1) * 2 + (i >> 1)] = lr128(l, wx + ((gr + 2) * 2 + (i >> 1)) * 1024);
      }
      SB();
    }
  }
}
DEV f32x4 colsum(const u32x8 (&w)[16], int set) {
  f32x4 c = zero4();
  UNR for (int i = 0; i < 16; ++i) c = mfma(half(w[8 * set + (i >> 1)], i & 1), ones4(), c);
  return c;
}
DEV unsigned timg_w(unsigned k, unsigned g) { return k * 64 + ((g ^ ((k >> 1) & 7)) << 3); }
DEV unsigned timg_r(int lane, int hf) {
  const unsigned k = 8 * (lane >> 4) + 4 * hf + ((lane & 15) >> 2);
  return k * 64 + (((lane & 3) ^ ((k >> 1) & 7)) << 3);
}
DEV int xn_flags(rsrc_t r, unsigned nr, int lane) { return (int)ld32(r, (lane & 7) * 128 + (lane & 8) * 512, flag_off(nr, FL_XN, 0)); }
template <int NP>
DEV void xn_fetch(rsrc_t r, lds_t l, unsigned nr, int sx, bool ok, int p0, int lane, unsigned l16, int& budget) {
  while (unlikely(!ok) && --budget >= 0) {
    ok = !any_lane(xn_flags(r, nr, lane) < sx);
    asm volatile("" ::: "memory");
    if (!ok) __builtin_amdgcn_s_sleep(2);
  }
  UNR for (int i = 0; i < NP; ++i) {
    const unsigned dst = (unsigned)(size_t)l + L_XS + ((sx & 1) ^ 1) * 32768 + (p0 + i) * 4096, src = ph(nr, V_XN + sx * X_SLOT + (p0 + i) * 4096);
    unsigned keep;
    asm volatile("s_mov_b32 %0, m0\n\ts_mov_b32 m0, %1\n\ts_nop 4\n\t"
                 "buffer_load_dwordx4 %2, %3, %4 offen sc1 lds\n\tbuffer_load_dwordx4 %2, %3, %4 offen offset:1024 sc1 lds\n\t"
                 "buffer_load_dwordx4 %2, %3, %4 offen offset:2048 sc1 lds\n\tbuffer_load_dwordx4 %2, %3, %4 offen offset:3072 sc1 lds\n\t"
                 "s_mov_b32 m0, %0"
                 : "=&s"(keep) : "s"(dst), "v"(l16), "s"(r), "s"(src) : "memory");
  }
}
DEV void stage1_bar(rsrc_t r, u32x4 a, unsigned voff, unsigned soff, unsigned la, int& budget) {
  asm volatile("s_waitcnt vmcnt(0)\n\tv_mov_b32 v248, %5\n\tv_mov_b32 v249, %6\n\tv_mov_b32 v250, %7\n\tv_mov_b32 v251, %8\n\ts_branch 2f\n"
               "1:\n\ts_add_i32 %0, %0, -1\n\ts_cmp_lt_i32 %0, 0\n\ts_cbranch_scc1 3f\n\t"
               "buffer_load_dwordx4 v[248:251], %1, %2, %3 offen sc1\n\ts_waitcnt vmcnt(0)\n"
               "2:\n\tds_write_b128 %4, v[248:251]\n\tv_max3_u32 v247, v248, v249, v250\n\tv_max_u32 v247, v247, v251\n\t"
               "v_cmp_eq_u32 vcc, -1, v247\n\ts_cbranch_vccnz 1b\n"
               "3:\n\ts_waitcnt lgkmcnt(0)\n\ts_barrier"
               : "+s"(budget) : "v"(voff), "s"(r), "s"(soff), "v"(la), "v"(a.x), "v"(a.y), "v"(a.z), "v"(a.w)
               : "memory", "scc", "vcc", "v247", "v248", "v249", "v250", "v251");
}
DEV void stage2_bar(rsrc_t r, u32x4 a, u32x4 b, unsigned voff, unsigned soff, unsigned la, int& budget) {
  asm volatile("s_waitcnt vmcnt(0)\n\tv_mov_b32 v248, %5\n\tv_mov_b32 v249, %6\n\tv_mov_b32 v250, %7\n\tv_mov_b32 v251, %8\n\t"
               "v_mov_b32 v252, %9\n\tv_mov_b32 v253, %10\n\tv_mov_b32 v254, %11\n\tv_mov_b32 v255, %12\n\ts_branch 2f\n"
               "1:\n\ts_add_i32 %0, %0, -1\n\ts_cmp_lt_i32 %0, 0\n\ts_cbranch_scc1 3f\n\t"
               "buffer_load_dwordx4 v[248:251], %1, %2, %3 offen sc1\n\tbuffer_load_dwordx4 v[252:255], %1, %2, %3 offen offset:1024 sc1\n\t"
               "s_waitcnt vmcnt(0)\n"
               "2:\n\tds_write_b128 %4, v[248:251]\n\tds_write_b128 %4, v[252:255] offset:1024\n\t"
               "v_max3_u32 v247, v248, v249, v250\n\tv_max3_u32 v246, v252, v253, v254\n\tv_max3_u32 v247, v247, v251, v255\n\t"
               "v_max_u32 v247, v247, v246\n\tv_cmp_eq_u32 vcc, -1, v247\n\ts_cbranch_vccnz 1b\n"
               "3:\n\ts_waitcnt lgkmcnt(0)\n\ts_barrier"
               : "+s"(budget) : "v"(voff), "s"(r), "s"(soff), "v"(la), "v"(a.x), "v"(a.y), "v"(a.z), "v"(a.w), "v"(b.x), "v"(b.y), "v"(b.z), "v"(b.w)
               : "memory", "scc", "vcc", "v246", "v247", "v248", "v249", "v250", "v251", "v252", "v253", "v254", "v255");
}
#define MEM_OFF(s, cp) ph(nr, V_MEM + ((cp) * 8 + ((s) & 7)) * BLK)
#define PM_OFF(s, h) ph(nr, V_PM + (((s) & 7) * 4 + ((h) >> 3)) * BLK + ((h) & 7) * 640)
#define PX_OFF(s, h) ph(nr, V_PX + ((s) & 7) * PX_SLOT + (h) * 2048)
#define Y_OFF(s, ct) ph(nr, V_Y + (((s) & 3) * 32 + (ct)) * BLK)
#define U_OFF(s, ct, h) ph(nr, V_U + ((s) & 3) * MB + (ct) * 32768 + (h) * 1024)

template <int KIND>
DEV void role_S(const u32x8 (&w)[16], rsrc_t r, lds_t l, int L, unsigned nr, int cp, int h, int tid, int wave, int lane) {
  constexpr unsigned L_KX = L_T, L_QX = L_T + 16384;
  constexpr int XT = KIND >= 1 && KIND <= 4 ? (KIND & 1) : -1, XM = (KIND - 1) >> 1;
  int budget = SPIN_BUDGET;
  TK_DECL
  const int c = lane & 15, g = lane >> 4;
  const unsigned l16 = opaque(lane * 16), a32 = opaque((g * 32 + c) * 16);
  const int sidx = (int)opaque(c < 8 ? 0x4444u : 0xEEEEu);
  const unsigned tw = opaque(timg_w(16 * wave + c, g)), tr0 = opaque(timg_r(lane, 0)), tr1 = opaque(timg_r(lane, 1));
  const unsigned mw = opaque(L_M0 + (16 * wave + c) * 32 + ((g ^ (c >> 2)) << 3));
  const unsigned mr0 = opaque(L_M0 + (8 * g + (c >> 2)) * 32 + (((c & 1) ^ (2 * g & 3)) << 3));
  const unsigned mr1 = opaque(L_M0 + (8 * g + 4 + (c >> 2)) * 32 + (((c & 1) ^ ((2 * g + 1) & 3)) << 3));
  const unsigned mk0 = opaque(mr0 ^ 16), mk1 = opaque(mr1 ^ 16);
  const unsigned po0 = opaque((g * 8 + (c & 7)) * 16 + (c >> 3) * 8), po1 = opaque(c * 16 + (g & 1) * 8 + (g >> 1) * 4);
  const unsigned po1m = opaque(c < 8 ? po1 + 512 : OOB), po2 = opaque(lane < 40 ? lane * 16 : OOB);
  const unsigned mla = opaque((unsigned)(size_t)l + L_MEM + (KIND == 1 ? 0 : wave * 1024) + l16), wx = opaque(L_W0 + XM * 16384 + l16);
  constexpr int NM = KIND == 1 ? 2 : 1;
#define MEM_LD { mv[0] = ldg(r, l16, mb); if (NM == 2) mv[NM - 1] = ldg(r, l16 + 1024, mb); }
  u32x4 mv[NM], xf[3][4];
  if (KIND == 0) { UNR for (int i = 0; i < 32; ++i) lw128(l, opaque(L_W0 + l16) + i * 1024, half(w[i >> 1], i & 1)); }
  if (L) xn_fetch<1>(r, l, nr, 0, false, wave, lane, l16, budget);
  if (tid == 0) *(LAS unsigned*)(l + L_FS) = 0;
  drain();
  bar();
  ST(5, 7);
  xn_fetch<1>(r, l, nr, 1, L == 0, wave, lane, l16, budget);
  if (KIND == 0) armed(r, nr, lane, budget);
  if (KIND) x_pre(l, 0, a32, xf, XT >= 0, wx);
  constexpr float SC = 0.08838834764831845f * 1.4426950408889634f, NEG = -1.0e9f / 0.08838834764831845f;
  unsigned tb = 0, dur = 0;
  for (int s = 0; s < NSEG; ++s) {
    const unsigned mb = pin_s(MEM_OFF(s, cp) + (KIND == 1 ? 0 : wave * 1024));
    if (KIND) {
      const int b = s & 1;
      f32x4 aq[2] = {zero4(), zero4()}, ak[2] = {zero4(), zero4()}, ax = zero4();
      int xfv = 0;
      x_gemm<true, XT, ABL_S>(l, b, a32, w, aq, ak, xf, [&](int gr) {
        if (gr == 5 && KIND == 2) xfv = xn_flags(r, nr, lane);
        if (gr == 7) MEM_LD;
      }, wx, ax);
      TK(7);
      UNR for (int t = 0; t < 2; ++t) {
        const unsigned o = opaque((tw ^ (t * 32)) + b * 8192);
        lw64(l, L_QX + o, pk4(aq[t]));
        lw64(l, L_KX + o, pk4(ak[t]));
        if (t == XT) lw64(l, (XM ? L_KX : L_QX) - KIND * 1024 + o, pk4(ax));
      }
      TK(8);
      if (KIND == 3) {
        const unsigned d = __builtin_amdgcn_readfirstlane(*(volatile LAS unsigned*)(l + L_FS)) - tb;
        dur = d > 150 ? GUESS * 85 : d;
      }
      if (KIND == 2) xn_fetch<0>(r, l, nr, s + 2 < NSEG ? s + 2 : s, !any_lane(xfv < (s + 2 < NSEG ? s + 2 : s)), 0, lane, l16, budget);
    }
    TK(9);
    ST(s - (s > 0), 4);
    STW(s - (s > 0), 5, 4); STW(s - (s > 0), 6, 7);
    ST(s, 0);
    const unsigned k0 = opaque(tr0 + (s & 1) * 8192), k1 = opaque(tr1 + (s & 1) * 8192), k2 = opaque(k0 ^ 32), k3 = opaque(k1 ^ 32);
    const unsigned o = pin_s(PM_OFF(s, h));
    TK(11);
    if (KIND) {
      if (POLL_PRIO) __builtin_amdgcn_s_setprio(POLL_PRIO);
      if (NM == 2) stage2_bar(r, mv[0], mv[NM - 1], l16, mb, mla, budget); else stage1_bar(r, mv[0], l16, mb, mla, budget);
      if (POLL_PRIO) __builtin_amdgcn_s_setprio(0);
    } else {
      bar();
    }
    TK(1);
    ST(s, 1);
    u32x4 f0[4], f1[4], fq[4], fm[4];
    {
      f32x4 aq = zero4(), ak = zero4();
      u32x4 f[8];
      unsigned long long rt;
      if (KIND == 3) asm volatile("s_memrealtime %0" : "=s"(rt)::"memory");
      UNR for (int i = 0; i < 8; ++i) f[i] = lr128(l, L_MEM + l16 + i * 1024);
      SB();
      UNR for (int i = 0; i < 4; ++i) { aq = smfmac(f[i], w[i], aq, sidx); ak = smfmac(f[i], w[8 + i], ak, sidx); }
      SB();
      if (KIND == 3) {
        asm volatile("s_waitcnt lgkmcnt(0)" : "+s"(rt)::"memory");
        tb = (unsigned)rt;
        bell(r, nr, FL_S, h, tb + dur, s, lane);
      }
      if (S_PRE == 2 && KIND >= 1 && KIND <= 3) x_pre(l, (s + 1) & 1, a32, xf, XT >= 0, wx);
      SB();
      UNR for (int i = 4; i < 8; ++i) { aq = smfmac(f[i], w[i], aq, sidx); ak = smfmac(f[i], w[8 + i], ak, sidx); }
      SB();
      if (KIND == 0) {
        UNR for (int ks = 0; ks < 4; ++ks) {
          f0[ks] = join2(lds_tr(l, L_KX + k0 + ks * 2048), lds_tr(l, L_KX + k1 + ks * 2048));
          f1[ks] = join2(lds_tr(l, L_KX + k2 + ks * 2048), lds_tr(l, L_KX + k3 + ks * 2048));
        }
        __builtin_amdgcn_s_setprio(3);
      }
      SB();
      lw64(l, mw, pk4(fold32(aq, ak)));
    }
    TK(2);
    if (S_PRE == 1 && KIND >= 1 && KIND <= 3) {
      SB();
      x_pre(l, (s + 1) & 1, a32, xf, XT >= 0, wx);
      asm volatile("s_waitcnt lgkmcnt(12)\n\ts_barrier" ::: "memory");
    } else {
      bar();
    }
    TK(3);
    ST(s, 2);
    if (KIND == 0) {
      {
        f32x4 am = zero4(), a0 = am, a1 = am;
        UNR for (int ks = 0; ks < 4; ++ks) fq[ks] = join2(lds_tr(l, mr0 + ks * 1024), lds_tr(l, mr1 + ks * 1024));
        UNR for (int ks = 0; ks < 4; ++ks) fm[ks] = join2(lds_tr(l, mk0 + ks * 1024), lds_tr(l, mk1 + ks * 1024));
        SB();
        UNR for (int ks = 0; ks < 4; ++ks) { a0 = mfma(f0[ks], fq[ks], a0); a1 = mfma(f1[ks], fq[ks], a1); }
        UNR for (int ks = 0; ks < 4; ++ks) am = mfma(fm[ks], fq[ks], am);
        f32x4 e = c < 8 ? a0 : a1;
        float em = g < 2 ? (c < 8 ? am[0] : am[1]) : (c < 8 ? am[2] : am[3]);
        float mx = fmaxn(fmaxn(fmaxn(e[0], e[1]), e[2]), fmaxn(e[3], em));
        mx = xmax(fmaxn(mx, ror8(mx))) * -SC;
        UNR for (int j = 0; j < 4; ++j) e[j] = __builtin_amdgcn_exp2f(__builtin_fmaf(e[j], SC, mx));
        em = __builtin_amdgcn_exp2f(__builtin_fmaf(em, SC, mx));
        float sum = ((e[0] + e[1]) + (e[2] + e[3])) + em;
        const float inv = __builtin_amdgcn_rcpf(xsum(sum + ror8(sum)));
        u32x2 px = pk4(e * inv);
        em *= inv;
        const unsigned pm = umin(pk(em, ror8(em)), SENT - 1);
        px.x = umin(px.x, SENT - 1); px.y = umin(px.y, SENT - 1);
        __builtin_amdgcn_raw_buffer_store_b64(px, r, po0, o, 16);
        __builtin_amdgcn_raw_buffer_store_b32(pm, r, po1m, o, 16);
        ST(s, 3);
        SB();
        *(LAS unsigned*)(l + L_FS) = now();
      }
      TK(4);
      {
        const unsigned ox = PX_OFF(s, h);
        u32x4 fb[4];
        UNR for (int ks = 0; ks < 4; ++ks) {
          fq[ks] = join2(lds_tr(l, L_QX + k0 + ks * 2048), lds_tr(l, L_QX + k1 + ks * 2048));
          fb[ks] = join2(lds_tr(l, L_QX + k2 + ks * 2048), lds_tr(l, L_QX + k3 + ks * 2048));
        }
        f32x4 a0 = zero4(), am = a0, b0 = a0, b1 = a0, bm = a0;
        UNR for (int ks = 0; ks < 4; ++ks) {
          a0 = mfma(f0[ks], fq[ks], a0); am = mfma(fm[ks], fq[ks], am);
          b0 = mfma(f0[ks], fb[ks], b0); b1 = mfma(f1[ks], fb[ks], b1); bm = mfma(fm[ks], fb[ks], bm);
        }
        UNR for (int j = 0; j < 4; ++j) { a0[j] = j <= c - 4 * g ? a0[j] : NEG; b1[j] = j <= c - 4 * g ? b1[j] : NEG; }
        f32x4 m = {g < 2 ? am[0] : am[2], g < 2 ? am[1] : am[3], g < 2 ? bm[0] : bm[2], g < 2 ? bm[1] : bm[3]};
        float xa = fmaxn(fmaxn(fmaxn(a0[0], a0[1]), fmaxn(a0[2], a0[3])), fmaxn(m[0], m[1]));
        float xb = fmaxn(fmaxn(fmaxn(fmaxn(b0[0], b0[1]), fmaxn(b0[2], b0[3])), fmaxn(m[2], m[3])), fmaxn(fmaxn(b1[0], b1[1]), fmaxn(b1[2], b1[3])));
        xa = xmax(xa) * -SC; xb = xmax(xb) * -SC;
        UNR for (int j = 0; j < 4; ++j) {
          a0[j] = __builtin_amdgcn_exp2f(__builtin_fmaf(a0[j], SC, xa)); m[j] = __builtin_amdgcn_exp2f(__builtin_fmaf(m[j], SC, j < 2 ? xa : xb));
          b0[j] = __builtin_amdgcn_exp2f(__builtin_fmaf(b0[j], SC, xb)); b1[j] = __builtin_amdgcn_exp2f(__builtin_fmaf(b1[j], SC, xb));
        }
        const float ia = __builtin_amdgcn_rcpf(xsum(((a0[0] + a0[1]) + (a0[2] + a0[3])) + (m[0] + m[1])));
        const float ib = __builtin_amdgcn_rcpf(xsum((((b0[0] + b0[1]) + (b0[2] + b0[3])) + (m[2] + m[3])) + ((b1[0] + b1[1]) + (b1[2] + b1[3]))));
        const u32x4 va = unsent(join2(pk4(a0 * ia), (u32x2){pk(m[0] * ia, m[1] * ia), pk(m[2] * ib, m[3] * ib)}));
        stg(r, l16, ox, unsent(join2(pk4(b0 * ib), pk4(b1 * ib))));
        __builtin_amdgcn_raw_buffer_store_b64((u32x2){va.x, va.y}, r, (l16 >> 1) + 1024, ox, 16);
        __builtin_amdgcn_raw_buffer_store_b32(va.z, r, po1 + 1536, ox, 16);
        __builtin_amdgcn_raw_buffer_store_b32(va.w, r, po1 + 1792, ox, 16);
      }
      __builtin_amdgcn_s_setprio(0);
    } else {
      if (KIND == 4) xn_fetch<DMA4>(r, l, nr, s + 2 < NSEG ? s + 2 : s, true, 0, lane, l16, budget);
      if (KIND == 5) xn_fetch<1>(r, l, nr, s + 2 < NSEG ? s + 2 : s, true, wave, lane, l16, budget);
      if (!S_PRE && KIND <= 3) __builtin_amdgcn_s_sleep(2);
      if (!S_PRE || KIND > 3) x_pre(l, (s + 1) & 1, a32, xf, XT >= 0, wx);
      TK(4);
      if (KIND == 4) {
        if (S_NAP) __builtin_amdgcn_s_sleep(S_NAP);
        stg(r, po2, PM_OFF(s + AHEAD, h), sent4());
        stg(r, l16, PX_OFF(s + AHEAD, h), sent4()); stg(r, l16 + 1024, PX_OFF(s + AHEAD, h), sent4());
        if (S_NAP2) __builtin_amdgcn_s_sleep(S_NAP2);
      }
    }
    TK(5);
  }
  drain();
  TK_DUMP(h);
}

template <bool isv>
DEV void role_V(const Params& p, rsrc_t r, lds_t l, int L, unsigned nr, int cp, int h, int tid, int wave, int lane) {
  constexpr unsigned L_VM = L_M0, L_VX = L_T;
  int budget = SPIN_BUDGET;
  const int w4 = wave & 3;
  u32x8 w[16];
  {
    u32x4 s0[16], s1[16];
    wblk_load<256>(s0, p.Wv + h * 128, NH * 128, tid);
    wblk_load<1024>(s1, p.Wo + (size_t)h * 128 * DM, DM, tid);
    wblk_put<256>(l, s0, tid); bar();
    if (isv) {
      UNR for (int t = 0; t < 2; ++t) {
        const unsigned wb = wblk_base<256>(2 * w4 + t, lane);
        UNR for (int i = 0; i < 8; ++i) w[8 * t + i] = wblk_pair<256>(l, wb, i);
      }
    }
    bar(); wblk_put<1024>(l, s1, tid); bar();
    if (!isv) {
      UNR for (int ct = 0; ct < 8; ++ct) {
        const unsigned wb = wblk_base<1024>(8 * w4 + ct, lane);
        UNR for (int i = 0; i < 2; ++i) w[2 * ct + i] = wblk_pair<1024>(l, wb, i);
      }
    }
    bar();
  }
  ST(4, 7);
  TK_DECL
  const int c = lane & 15, g = lane >> 4;
  const unsigned l16 = opaque(lane * 16), a32 = opaque((g * 32 + c) * 16);
  const int sidx = (int)opaque(c < 8 ? 0x4444u : 0xEEEEu);
  const unsigned tw = opaque(timg_w(32 * w4 + c, g)), tr0 = opaque(timg_r(lane, 0)), tr1 = opaque(timg_r(lane, 1));
  const unsigned vmw = opaque(L_VM + (32 * w4 + 16 * (g >> 1) + c) * 16 + (g & 1) * 8);
  const unsigned vmr = opaque(L_VM + (16 * g + ((c >> 2) & 1) + 4 * (c >> 3) + (c & 2)) * 16 + (c & 1) * 8);
  const unsigned mla = opaque((unsigned)(size_t)l + L_MEM + w4 * 2048 + l16);
  const unsigned yvo = opaque(((g >> 1) + 2 * (g & 1)) * 2 * BLK + c * 16);
  u32x4 m[2];
  auto v_phase = [&](int sx, int sn, int sm) {
    const int b = sx & 1;
    u32x4 xf[3][4];
    const int xfv = xn_flags(r, nr, lane);
    x_pre(l, b, a32, xf);
    if (V_NAP) __builtin_amdgcn_s_sleep(V_NAP);
    TK(5);
    f32x4 a0[2] = {zero4(), zero4()}, a1[2] = {zero4(), zero4()};
    x_gemm<true, -1, ABL_V>(l, b, a32, w, a0, a1, xf, [&](int gr) {
      if (gr == V_DMA || gr == V_DMA + 1) xn_fetch<1>(r, l, nr, sn, gr > V_DMA || (L == 0 && sn == 1) || !any_lane(xfv < sn), 2 * w4 + gr - V_DMA, lane, l16, budget);
      if (gr == 7) { const unsigned mb = MEM_OFF(sm, cp) + w4 * 2048; m[0] = ldg(r, l16, mb); m[1] = ldg(r, l16 + 1024, mb); }
    }, 0u, a0[0]);
    TK(7);
    UNR for (int t = 0; t < 2; ++t) {
      const unsigned o = opaque((tw ^ (t * 32)) + b * 8192);
      lw64(l, L_VX + o, pk4(a0[t]));
      lw64(l, L_VX + o + 1024, pk4(a1[t]));
    }
    TK(8);
  };
  auto u_phase = [&](int sx) {
    const unsigned k0 = opaque(tr0 + (sx & 1) * 8192), k1 = opaque(tr1 + (sx & 1) * 8192);
    u32x4 vf[2][4];
    UNR for (int ks = 0; ks < 4; ++ks)
      UNR for (int t = 0; t < 2; ++t) vf[t][ks] = join2(lds_tr(l, L_VX + (k0 ^ (t * 32)) + ks * 2048), lds_tr(l, L_VX + (k1 ^ (t * 32)) + ks * 2048));
    const unsigned ub = U_OFF(sx, 8 * w4, h);
    if (U_NAP) __builtin_amdgcn_s_sleep(U_NAP);
    SB();
    f32x4 a[2][2];
    u32x4 v;
    UNR for (int ct = 0; ct <= 8; ++ct) {
      UNR for (int ks = 0; ks < 4; ++ks) {
        if (ct < 8) {
          const u32x4 b = half(w[2 * ct + (ks >> 1)], ks & 1);
          UNR for (int t = 0; t < 2; ++t) a[ct & 1][t] = mfma(vf[t][ks], b, ks ? a[ct & 1][t] : zero4());
        }
        SB();
        if (ct && (ks == 1 || ks == 2)) { const u32x2 h2 = pk4(a[(ct - 1) & 1][ks - 1]); v[2 * ks - 2] = h2.x; v[2 * ks - 1] = h2.y; }
        if (ct && ks == 3) stg(r, l16, ub + (ct - 1) * 65536, v);
        SB();
      }
    }
    __builtin_amdgcn_s_setprio(0);
    if (UEARLY) { drain(); flag_st(r, flag_off(nr, FL_U + w4, h), sx, lane); }
  };
  unsigned tb = 0, dur = GUESS * 72;
  auto m_phase = [&](int s, int ud) {
    ST(s, 0);
    TK(11);
    const unsigned yo = pin_s(Y_OFF(s, 8 * w4) + h * 256);
    f32x4 y[8];
    if (!isv) {
      UNR for (int ct = 0; ct < 8; ++ct) { y[ct] = zero4(); asm volatile("" : "+v"(y[ct])); }
    }
    if (isv) {
      const unsigned mb = pin_s(MEM_OFF(s, cp) + w4 * 2048);
      if (POLL_PRIO) __builtin_amdgcn_s_setprio(POLL_PRIO);
      stage2_bar(r, m[0], m[1], l16, mb, mla, budget);
    } else {
      bar();
    }
    TK(1);
    ST(s, 1);
    if (isv) {
      f32x4 a0 = zero4(), a1 = a0;
      u32x4 f[8];
      UNR for (int i = 0; i < 8; ++i) f[i] = lr128(l, L_MEM + l16 + i * 1024);
      SB();
      UNR for (int i = 0; i < 8; ++i) { a0 = smfmac(f[i], w[i], a0, sidx); a1 = smfmac(f[i], w[8 + i], a1, sidx); }
      lw64(l, vmw, pk4(fold32(a0, a1)));
      if (POLL_PRIO) __builtin_amdgcn_s_setprio(0);
    } else {
      tb = now();
      if (!UEARLY) drain();
      bell(r, nr, FL_V, h, tb + dur, s, lane | w4);
      if (!UEARLY) flag_st(r, flag_off(nr, FL_U + w4, h), ud, lane);
      __builtin_amdgcn_s_setprio(3);
    }
    TK(2);
    bar();
    TK(3);
    ST(s, 2);
    if (!isv) {
      u32x4 f[2];
      UNR for (int i = 0; i < 2; ++i) f[i] = join2(lds_tr(l, vmr + i * 1024), lds_tr(l, vmr + i * 1024 + 128));
      SB();
      u32x2 pp[4];
      UNR for (int q = 0; q <= 4; ++q) {
        f32x4 sw0, sw1;
        UNR for (int i = 0; i < 4; ++i) {
          if (q < 4) y[2 * q + (i & 1)] = smfmac(f[i >> 1], w[4 * q + 2 * (i & 1) + (i >> 1)], y[2 * q + (i & 1)], sidx);
          SB();
          if (q && i == 1) {
            UNR for (int j = 0; j < 4; ++j) {
              auto t = __builtin_amdgcn_permlane32_swap(f2u(y[2 * q - 2][j]), f2u(y[2 * q - 1][j]), false, false);
              sw0[j] = u2f(t[0]); sw1[j] = u2f(t[1]);
            }
          }
          if (q && i == 2) sw0 += sw1;
          if (q && i == 3) { pp[q - 1] = pk4(sw0); asm volatile("" : "+v"(pp[q - 1])); }
          if (q == 3 && i == 0) stg(r, yvo, yo, unsent(pair16(pp[0], pp[1])));
          SB();
        }
      }
      stg(r, yvo, yo + 8 * BLK, unsent(pair16(pp[2], pp[3])));
      STW(s, 3, 4);
      dur = since(tb, 150);
      __builtin_amdgcn_s_setprio(U_PRIO);
      TK(4);
    }
  };
  if (!isv) armed(r, nr, lane, budget);
  if (L) { xn_fetch<1>(r, l, nr, 0, false, wave, lane, l16, budget); drain(); bar(); }
  ST(5, 7);
  for (int k = 0; k < NSEG + 3; ++k) {
    const int sn = k + 1 < NSEG ? k + 1 : k - 1 < NSEG ? k - 1 : k - 3;
    if (likely(k > 3 || k == 2)) m_phase(k == 2 ? 0 : k - 3, k - 2);
    else {
      if (k) { drain(); if (!isv) flag_st(r, flag_off(nr, FL_U + w4, h), k - 2, lane); bar(); }
    }
    if (isv) { __builtin_amdgcn_s_sleep(1); v_phase(k, sn, k < 2 ? 0 : k < 4 ? 1 : k - 2); ST(k < 3 ? 0 : k - 3, 5); }
    else if (likely(k)) {
      STW(k < 3 ? 0 : k - 3, 4, 4); u_phase(k - 1); STW(k < 3 ? 0 : k - 3, 6, 4);
      if (unlikely(k < 3)) { drain(); flag_st(r, flag_off(nr, FL_U + w4, h), k - 1, lane); }
    }
    TK(10);
  }
  drain();
  TK_DUMP(32 + h);
}

DEV void role_R(const Params& p, rsrc_t r, lds_t l, int L, unsigned nr, int rep, int rc, int tid, int wave, int lane) {
  constexpr unsigned L_RC = 0, L_RX = 8192, CPT = 22;
  int budget = SPIN_BUDGET;
  const int c = lane & 15, g = lane >> 4;
  const unsigned l16 = opaque(lane * 16), pxo = opaque((c >> 3) * 640 + (g * 8 + (c & 7)) * 16), pmo = opaque(c < 8 ? g * 640 + 512 + c * 16 : OOB);
  const unsigned zo = opaque(g * 256 + (((c & 6) << 1) | ((c >> 3) << 1) | (c & 1)) * 16);
  const unsigned xwo = opaque(c * 16 + (g & 1) * 512 + (g >> 1) * 8);
  const unsigned xmo = opaque(g * 2048 + 1536 + c * 16), xvo = opaque(g & 1 ? OOB : ((g >> 1) * 32 + c) * 16);
  const unsigned mro = opaque((((lane >> 5) * 2 + ((lane >> 1) & 1)) * 16 + (wave & 1) * 8 + ((lane >> 2) & 7)) * 16 + (lane & 1) * 8);
  const unsigned l4 = opaque(lane * 4), bo = opaque((lane & 3) * 128 + (lane & 4) * 1024);
  unsigned bm = (lane & 4 ? Y_EXTRA : BELL) << 7;
  UNR for (int i = 0; i < 4; ++i) stg(r, l16, Y_OFF(rep + (i & 2), 2 * rc + (i & 1)) + wave * 1024, sent4());
  {
    const unsigned id = rep * 16 + rc, t16 = opaque(tid * 16);
    stg(r, t16, ph(nr, V_PX + id * BLK), sent4());
    stg(r, t16, ph(nr, (id < 16 ? V_X1 : V_X2 - 16 * BLK) + id * BLK), sent4());
    stg(r, t16, ph(nr, V_H + id * BLK), sent4());
    if (id < 24) stg(r, t16, ph(nr, V_H + (32 + id) * BLK), sent4());
  }
  drain();
  bar();
  if (wave == 0) flag_st(r, flag_off(nr, FL_ARM + rep, rc), 0, lane);
  armed(r, nr, lane, budget);
  TK_DECL
  unsigned tgo = 0, xdur = GUESS * 160;
  u32x4 uf[4][2], un[4][2];
  auto u_load = [&](int s) {
    for (;;) {
      const int f = (int)ld32(r, (lane & 3) * 128, flag_off(nr, FL_U + (rc >> 2), 4 * wave));
      asm volatile("" ::: "memory");
      if (!any_lane(f < s) || --budget < 0) break;
      __builtin_amdgcn_s_sleep(1);
    }
    if (!UEARLY && UGATE && s) bell_wait(bell_get<4>(r, 0, flag_off(nr, FL_S, rc), SENT, s - 1, budget), s - 1, UGATE);
    TK(0);
    UNR for (int ct = 0; ct < 2; ++ct)
      UNR for (int hh = 0; hh < 4; ++hh) un[hh][ct] = ldg(r, zo + hh * 1024, U_OFF(s, 2 * rc + ct, 4 * wave));
  };
  for (int s = rep; s < NSEG; s += 2) {
    ST(s, 0);
    if (!UEARLY || s == rep) u_load(s);
    UNR for (int ct = 0; ct < 2; ++ct)
      UNR for (int hh = 0; hh < 4; ++hh) uf[hh][ct] = un[hh][ct];
    const unsigned xso = s * X_SLOT + (2 * rc + (wave & 1)) * 1024 + ((wave >> 1) & 1) * 256;
    const u32x4 res = L ? ldg(r, xvo, ph(nr, V_X2 + xso))
                        : *(const u32x4*)(p.x + (size_t)(32 * s + 16 * ((wave >> 1) & 1) + c) * DM + 32 * rc + 16 * (wave & 1) + 8 * (g >> 1));
    u32x4 q[5];
    const unsigned yb = pin_s(Y_OFF(s, 2 * rc) + wave * 1024), yb1 = pin_s(yb + 2 * BLK), pmb = pin_s(PM_OFF(s, 4 * wave));
    const unsigned mo = pin_s(MEM_OFF(s + 1, 0) + rc * 512 + (wave & 1) * 256);
    {
      unsigned bv;
      unsigned long long rt, ck;
      for (;;) {
        bv = ld32(r, bo, flag_off(nr, FL_S, 4 * wave));
        asm volatile("s_memrealtime %0\n\ts_memtime %1\n\ts_waitcnt lgkmcnt(0)" : "=&s"(rt), "=&s"(ck)::"memory");
        if (bell_rung(bv, s) || --budget < 0) break;
        if (CALM > 1) __builtin_amdgcn_s_sleep(2);
      }
      const unsigned d = (bv + bm - ((unsigned)rt << 7)) >> 7, due = (unsigned)ck + (d < 400u ? d * CPT : 0u);
      unsigned pend;
      do { pend = (unsigned)__builtin_amdgcn_ballot_w64((int)(due - (unsigned)__builtin_amdgcn_s_memtime()) > 0); } while ((pend & 0xf0) && (pend & 0x0f));
      asm volatile("" ::: "memory");
      if (!(pend & 0xf0)) {
        q[3] = ldg(r, zo, yb); q[4] = ldg(r, zo, yb1);
        asm volatile("" ::: "memory");
        while (__builtin_amdgcn_ballot_w64((int)(due - (unsigned)__builtin_amdgcn_s_memtime()) > 0) & 0x0f) {}
        asm volatile("" ::: "memory");
        q[0] = ldg(r, pxo, pmb); q[1] = ldg(r, pxo + 1280, pmb); q[2] = ldg(r, pmo, pmb);
      } else {
        q[0] = ldg(r, pxo, pmb); q[1] = ldg(r, pxo + 1280, pmb); q[2] = ldg(r, pmo, pmb);
        asm volatile("" ::: "memory");
        while (__builtin_amdgcn_ballot_w64((int)(due - (unsigned)__builtin_amdgcn_s_memtime()) > 0) & 0xf0) {}
        asm volatile("" ::: "memory");
        q[3] = ldg(r, zo, yb); q[4] = ldg(r, zo, yb1);
      }
      ST(s, 1);
    }
    TK(1);
    auto crit = [&]() {
      f32x4 lo[2], hi[2];
      UNR for (int ct = 0; ct < 2; ++ct) { lo[ct] = mfma(uf[0][ct], q[0], zero4()); hi[ct] = mfma(uf[1][ct], q[0], zero4()); }
      UNR for (int ct = 0; ct < 2; ++ct) { hi[ct] = mfma(uf[3][ct], q[1], hi[ct]); lo[ct] = mfma(uf[2][ct], q[1], lo[ct]); }
      lo[0] = mfma(q[3], q[2], lo[0]); lo[1] = mfma(q[4], q[2], lo[1]);
      f32x4 a;
      UNR for (int j = 0; j < 4; ++j) {
        const float own = c < 8 ? lo[0][j] : hi[1][j], oth = c < 8 ? lo[1][j] : hi[0][j];
        a[j] = own + ror8(oth);
      }
      lw128f(l, L_RC + wave * 1024 + l16, a);
      SB();
    };
    crit();
    if (unlikely(!all_there(q))) {
      POLL(q, 0, { TKC(10); q[3] = ldg(r, zo, yb); q[4] = ldg(r, zo, yb1); q[0] = ldg(r, pxo, pmb); q[1] = ldg(r, pxo + 1280, pmb); q[2] = ldg(r, pmo, pmb); });
      crit();
    }
    const u32x4 yf[2] = {q[3], q[4]};
    ST(s, 2);
    TK(3);
    bar();
    TK(4);
    ST(s, 3);
    if (likely(wave < 2)) {
      f32x2 t[8];
      UNR for (int w = 0; w < 8; ++w) t[w] = *(LAS f32x2*)(l + L_RC + mro + w * 1024);
      const f32x2 m = ((t[0] + t[1]) + (t[2] + t[3])) + ((t[4] + t[5]) + (t[6] + t[7]));
      UNR for (int i = 0; i < NCOPY; ++i) __builtin_amdgcn_raw_buffer_store_b32(umin(pk(m[0], m[1]), SENT - 1), r, l4, mo + i * 16 * BLK, 16);
      ST(s, 4);
      UNR for (int i = 0; i < NCOPY; ++i) __builtin_amdgcn_raw_buffer_store_b32(SENT, r, l4, (mo ^ (8 * BLK)) + i * 16 * BLK, 16);
    }
    if (wave == 2) {
      tgo = now();
      bell(r, nr, FL_RX + rep, rc, tgo + xdur, s, lane);
    } else if (wave >= 2) {
      if (MINI) STM(s, 3, 3);
      __builtin_amdgcn_s_sleep(5);
    }
    if (PX_NAP) __builtin_amdgcn_s_sleep(PX_NAP);
    TK(5);
    u32x4 px[8];
    {
      const unsigned b = PX_OFF(s, 4 * wave);
      POLL(px, 1, {
        TKC(11);
        UNR for (int i = 0; i < 4; ++i) px[i] = ldg(r, l16 + (i & 1) * 2048, b + (i >> 1) * 4096);
        UNR for (int i = 0; i < 2; ++i)
          px[4 + i] = join2(__builtin_amdgcn_raw_buffer_load_b64(r, (l16 >> 1) + 1024, b + i * 4096, 16), __builtin_amdgcn_raw_buffer_load_b64(r, (l16 >> 1) + 3072, b + i * 4096, 16));
        px[6] = ldg(r, xmo, b); px[7] = ldg(r, xmo + 256, b); });
    }
    ST(s, 5);
    TK(6);
    UNR for (int ct = 0; ct < 2; ++ct) {
      f32x4 a0 = mfma(yf[ct], px[6], zero4()), a1 = mfma(yf[ct], px[7], zero4()), b0 = zero4();
      UNR for (int hh = 0; hh < 4; ++hh) {
        a1 = mfma(uf[hh][ct], px[hh], a1);
        const u32x4 ph_ = px[4 + (hh >> 1)];
        b0 = mfma16((u32x2){uf[hh][ct].x, uf[hh][ct].y}, hh & 1 ? (u32x2){ph_.z, ph_.w} : (u32x2){ph_.x, ph_.y}, b0);
      }
      a0 += b0;
      lw64(l, L_RX + (wave * 4 + ct) * 1024 + xwo, (u32x2){f2u(a0[0]), f2u(a0[1])});
      lw64(l, L_RX + (wave * 4 + ct) * 1024 + xwo + 256, (u32x2){f2u(a0[2]), f2u(a0[3])});
      lw64(l, L_RX + (wave * 4 + 2 + ct) * 1024 + xwo, (u32x2){f2u(a1[0]), f2u(a1[1])});
      lw64(l, L_RX + (wave * 4 + 2 + ct) * 1024 + xwo + 256, (u32x2){f2u(a1[2]), f2u(a1[3])});
    }
    if (UEARLY == 1 && wave >= 4 && s + 2 < NSEG) u_load(s + 2);
    TK(7);
    bar();
    TK(8);
    if (wave < 4) {
      const u32x2 m = pk4(sum8(l, L_RX + wave * 1024 + l16, 4096));
      stg(r, xvo, ph(nr, V_X1 + xso), unsent(add_bf8(res, pair16(m, m))));
      ST(s, 6);
      xdur = since(tgo, 300);
      if (s + AHEAD < NSEG) stg(r, xvo, ph(nr, V_X1 + xso + AHEAD * X_SLOT), sent4());
    }
    if (UEARLY && (UEARLY == 2 || wave < 4) && s + 2 < NSEG) u_load(s + 2);
    stg(r, l16, yb, sent4()); stg(r, l16, yb1, sent4());
    TK(9);
  }
  TK_DUMP(64 + rep * 16 + rc);
}

DEV void role_F1(const Params& p, rsrc_t r, lds_t l, int L, unsigned nr, int f, int tid, int wave, int lane) {
  int budget = SPIN_BUDGET;
  u32x8 w[16];
  load_w2(w, l, p.Wg + f * 128, p.Wu + f * 128, FF, tid, wave, lane);
  const f32x4 cg = colsum(w, 0), cu = colsum(w, 1);
  const unsigned l16 = opaque(lane * 16), a32 = opaque(((lane >> 4) * 32 + (lane & 15)) * 16);
  const unsigned fso = opaque(L_FS + wave * 256 + (lane & 15) * 4), bo = opaque((lane & 1) * 128);
  unsigned bv = SENT, tb = 0, dur = GUESS * 135;
  armed(r, nr, lane, budget);
  for (int s = 0; s < NSEG; ++s) {
    const int b = s & 1;
    ST(s, 0);
    const unsigned xb = pin_s(ph(nr, V_X1 + s * X_SLOT + wave * 4096)), ho = pin_s(ph(nr, V_H + s * H_SLOT + (f * 16 + 2 * wave) * 512));
    bell_wait(bell_get<2>(r, bo, flag_off(nr, FL_RX + b, 2 * wave), bv, s, budget), s, XBELL);
    u32x4 xv[4];
    POLL(xv, 1, UNR for (int i = 0; i < 4; ++i) xv[i] = ldg(r, l16 + i * 1024, xb));
    x_put(l, b, xv, wave, l16);
    bar();
    ST(s, 1);
    if (wave == 7) { tb = now(); bell(r, nr, FL_F1, f, tb + dur, s, lane); }
    if (wave >= 4) x_final(l, b, wave, l16); else __builtin_amdgcn_s_setprio(1);
    f32x4 ag[2] = {zero4(), zero4()}, au[2] = {zero4(), zero4()};
    u32x4 xf[3][4];
    x_pre(l, b, a32, xf);
    x_gemm<false, -1>(l, b, a32, w, ag, au, xf, [&](int gr) { if (gr == 4) bv = ld32(r, bo, flag_off(nr, FL_RX + (b ^ 1), 2 * wave)); }, 0u, ag[0]);
    if (wave < 4) { __builtin_amdgcn_s_setprio(0); x_final(l, b, wave, l16); }
    u32x2 hp[2];
    UNR for (int t = 0; t < 2; ++t) {
      const float rs = *(LAS float*)(l + fso + t * 64), mr = *(LAS float*)(l + fso + t * 64 + 128);
      const u32x2 gt = pk4(ag[t] * rs + cg * mr), ut = pk4(au[t] * rs + cu * mr);
      UNR for (int j = 0; j < 2; ++j) {
        const unsigned gw = opaque(gt[j]), uw = opaque(ut[j]);
        hp[t][j] = pk(fmaxn(bflo(gw), 0.f) * bflo(uw), fmaxn(bfhi(gw), 0.f) * bfhi(uw));
      }
    }
    stg(r, l16, ho, unsent(pair16(hp[0], hp[1])));
    STW(s, 2, 7);
    if (wave == 7) dur = since(tb, 250);
    if (s + AHEAD < NSEG) stg(r, l16, ph(nr, V_H + (s + AHEAD) * H_SLOT + (f * 16 + 2 * wave) * 512), sent4());
  }
}

DEV void role_F2(const Params& p, rsrc_t r, lds_t l, int L, unsigned nr, int rt, int cs, int tid, int wave, int lane) {
  int budget = SPIN_BUDGET;
  const int c = lane & 15, g = lane >> 4;
  u32x4 wd[7][4];
  {
    u32x4 st[28];
    const __hip_bfloat16* base = p.Wd + (size_t)(224 * wave) * DM + 64 * cs;
    UNR for (int i = 0; i < 28; ++i) st[i] = *(const u32x4*)(base + (size_t)(8 * i + (lane >> 3)) * DM + (lane & 7) * 8);
    lds_t lw = l + wave * 16384;
    const unsigned q = (lane & 15) >> 2, pp = lane & 3;
    UNR for (int hf = 0; hf < 2; ++hf) {
      const int n = hf ? 12 : 16;
      UNR for (int i = 0; i < 16; ++i) if (i < n) {
        const unsigned row = 8 * i + (lane >> 3), sw = ((row >> 1) & 1) | ((row >> 2) & 2);
        lw128(lw, row * 128 + (((lane & 7) ^ (sw << 1)) << 4), st[hf * 16 + i]);
      }
      bar();
      UNR for (int k = 0; k < 4; ++k) if (hf * 4 + k < 7) {
        UNR for (int ct = 0; ct < 4; ++ct) {
          const unsigned r0 = 32 * k + 8 * g + q, r1 = r0 + 4;
          const unsigned s0 = ((r0 >> 1) & 1) | ((r0 >> 2) & 2), s1 = ((r1 >> 1) & 1) | ((r1 >> 2) & 2);
          wd[hf * 4 + k][ct] = join2(lds_tr(lw, r0 * 128 + ((ct ^ s0) << 5) + pp * 8), lds_tr(lw, r1 * 128 + ((ct ^ s1) << 5) + pp * 8));
        }
      }
      bar();
    }
  }
  const unsigned l16 = opaque(lane * 16), a32 = opaque((g * 32 + 16 * rt + c) * 16), xvo = opaque(g & 1 ? OOB : ((g >> 1) * 32 + 16 * rt + c) * 16);
  const unsigned bo = opaque(umin(lane & 15, 13) * 128), nr1 = ~nr;
  unsigned tb = 0, dur = GUESS * 150;
  armed(r, nr, lane, budget);
  armed(r, nr1, lane, budget);
  for (int s = 0; s < NSEG; ++s) {
    ST(s, 0);
    const unsigned bv = bell_get<2>(r, bo, flag_off(nr, FL_F1, 0), SENT, s, budget);
    if (L == 0 && wave == 0) { tb = __builtin_amdgcn_readfirstlane(bv); tb = (tb & 127) == (unsigned)s ? tb >> 7 : now(); bell(r, nr1, FL_F2, rt * 8 + cs, tb + dur, s, lane); }
    const unsigned xso = s * X_SLOT + (8 * cs + 2 * (wave & 3)) * 512;
    u32x4 res[1] = {ldg(r, xvo, ph(nr, V_X1 + xso))};
    unsigned hb[7];
    UNR for (int k = 0; k < 7; ++k) hb[k] = pin_s(ph(nr, V_H + s * H_SLOT + (28 * wave + 4 * k) * 512));
    bell_wait(bv, s, XBELL);
    ST(s, 1);
    u32x4 hf[7];
    f32x4 a[4];
    POLL(hf, 1, {
      UNR for (int k = 0; k < 7; ++k) hf[k] = ldg(r, a32, hb[k]);
      UNR for (int k = 0; k < 7; ++k)
        UNR for (int ct = 0; ct < 4; ++ct) a[ct] = mfma(wd[k][ct], hf[k], k ? a[ct] : zero4());
    });
    if (unlikely(budget < 0 || !all_there(res))) res[0] = ldg(r, xvo, ph(nr, V_X1 + xso));
    ST(s, 2);
    const unsigned wo_ = opaque(l16 + (s & 1) * 32768 + wave * 4096), ro_ = opaque(l16 + (s & 1) * 32768 + wave * 1024);
    UNR for (int ct = 0; ct < 4; ++ct) lw128f(l, wo_ + ct * 1024, a[ct]);
    bar();
    if (wave < 4) {
      const u32x2 m = pk4(sum8(l, ro_, 4096));
      const u32x4 v = add_bf8(res[0], pair16(m, m));
      if (L == 0) {
        stg(r, xvo, ph(nr1, V_X2 + xso), unsent(v));
        dur = since(tb, 250);
        if (s + AHEAD < NSEG) stg(r, xvo, ph(nr1, V_X2 + xso + AHEAD * X_SLOT), sent4());
      } else if (!(g & 1)) {
        *(u32x4*)(p.out + (size_t)(32 * s + 16 * rt + c) * DM + 64 * cs + 16 * wave + 8 * (g >> 1)) = v;
      }
    }
    ST(s, 3);
  }
}

DEV void role_N(const Params& p, rsrc_t r, lds_t l, int L, unsigned nr, int rt, int tid, int wave, int lane) {
  int budget = SPIN_BUDGET;
  const int c = lane & 15, g = lane >> 4;
  const unsigned a32 = opaque((g * 32 + 16 * rt + c) * 16);
  if (L) armed(r, nr, lane, budget);
  for (int s = L ? 0 : 2; s < NSEG; ++s) {
    u32x4 xv[2];
    ST(s, 0);
    if (L) {
      const unsigned xb = pin_s(ph(nr, V_X2 + s * X_SLOT + wave * 4096));
      bell_wait(bell_get<2>(r, 0, flag_off(nr, FL_F2, rt * 8 + wave), SENT, s, budget), s, XBELL);
      POLL(xv, 1, { xv[0] = ldg(r, a32, xb); xv[1] = ldg(r, a32 + 2048, xb); });
    } else {
      UNR for (int i = 0; i < 2; ++i) xv[i] = *(const u32x4*)(p.x + (size_t)(32 * s + 16 * rt + c) * DM + 64 * wave + 32 * i + 8 * g);
    }
    ST(s, 1);
    float s1 = 0.f, s2 = 0.f;
    UNR for (int i = 0; i < 2; ++i)
      UNR for (int j = 0; j < 4; ++j) { s1 = dot2(xv[i][j], 0x3F803F80u, s1); s2 = dot2(xv[i][j], xv[i][j], s2); }
    *(LAS f32x2*)(l + (s & 1) * 4096 + tid * 8) = (f32x2){s1, s2};
    const unsigned xo = pin_s(ph(nr, V_XN + s * X_SLOT + wave * 4096));
    bar();
    f32x2 t[8];
    UNR for (int i = 0; i < 8; ++i) t[i] = *(LAS f32x2*)(l + (s & 1) * 4096 + i * 512 + lane * 8);
    const f32x2 u = ((t[0] + t[1]) + (t[2] + t[3])) + ((t[4] + t[5]) + (t[6] + t[7]));
    const float mean = xsum(u[0]) * (1.f / DM);
    const float rstd = __builtin_amdgcn_rsqf(__builtin_fmaxf(xsum(u[1]) * (1.f / DM) - mean * mean, 0.f) + 1e-5f);
    UNR for (int i = 0; i < 2; ++i) {
      u32x4 o;
      UNR for (int j = 0; j < 4; ++j) o[j] = pk((bflo(xv[i][j]) - mean) * rstd, (bfhi(xv[i][j]) - mean) * rstd);
      stg(r, a32 + i * 2048, xo, o);
    }
    drain();
    flag_st(r, flag_off(nr, FL_XN + rt, wave), s, lane);
    ST(s, 2);
  }
}

__global__ void __launch_bounds__(512, 2) rmt_mega(Params p) {
  extern __shared__ __attribute__((aligned(16))) unsigned char smem[];
  lds_t l = (lds_t)smem;
  const int tid = __builtin_amdgcn_workitem_id_x(), lane = tid & 63, wave = __builtin_amdgcn_readfirstlane(tid >> 6);
  const rsrc_t r = __builtin_amdgcn_make_buffer_rsrc(p.ws, 0, RS_END, 0x00020000);
  const int b0 = __builtin_amdgcn_workgroup_id_x();
  const unsigned cal = *(const unsigned*)(p.ws + WS_CAL + umin(lane, NREG - 1) * 128);
  const int b = (b0 & ~7) | ((b0 + (__builtin_amdgcn_readfirstlane(cal) >> 4)) & 7), L = (b >> 2) & 1;
  const unsigned nr = (unsigned)__builtin_amdgcn_ballot_w64(cal & 1) ^ (L ? ~0u : 0u) ^ NEAR_FLIP;
  const int xq = ((L ? XPERM1 : XPERM) >> (4 * (b & 3))) & 3, j = b >> 3, cp = xq & (NCOPY - 1);
  const int idx = MAP == 0 ? xq + 4 * j
                : MAP == 6 ? (xq < 2 ? xq * 16 + (j & 15) + (j >> 4) * 32 : (j < 16 ? 64 : 80) + (xq - 2) * 16 + j)
                : MAP == 9 ? (xq < 2 ? xq * 16 + (j & 15) + (j >> 4) * 32 : (j < 16 ? 64 + (xq - 2) + 2 * j : 80 + (xq - 2) * 16 + j))
                : MAP == 13 ? (xq < 2 ? (j < 16 ? xq * 16 + j : 64 + xq + 2 * (j - 16)) : 32 + (xq - 2) * 16 + (j & 15) + (j >> 4) * 64)
                : MAP == 14 ? (xq < 2 ? xq * 16 + (j & 15) + (j >> 4) * 96 : (j < 16 ? 32 + (xq - 2) * 16 + j : 64 + (xq - 2) + 2 * (j - 16)))
                : MAP == 10 ? (xq < 2 ? xq * 16 + (j & 15) + (j >> 4) * 64 : 32 + (xq - 2) * 16 + (j & 15) + (j >> 4) * 64)
                            : (xq < 2 ? xq * 16 + (j & 15) + (j >> 4) * 96 : 32 + (xq - 2) * 16 + (j & 15) + (j >> 4) * 32);
  if (L == 0 && idx < 64) { int budget = 0; xn_fetch<1>(r, l, nr, 0, true, wave, lane, lane * 16, budget); }
  if (idx < 64 || (idx >= 96 && idx < 126)) { if (L || idx >= 96) nap_us(DLY_O); else if (idx < 32) nap_us(DLY_S); }
  if (idx < 32) {
    u32x8 w[16];
    load_w2(w, l, p.Wq + idx * 128, p.Wk + idx * 128, NH * 128, tid, wave, lane);
    ST(4, 7);
    if (wave == 0) role_S<0>(w, r, l, L, nr, cp, idx, tid, wave, lane);
    else if (wave == 1) role_S<1>(w, r, l, L, nr, cp, idx, tid, wave, lane);
    else if (wave == 2) role_S<2>(w, r, l, L, nr, cp, idx, tid, wave, lane);
    else if (wave == 3) role_S<3>(w, r, l, L, nr, cp, idx, tid, wave, lane);
    else if (wave == 4) role_S<4>(w, r, l, L, nr, cp, idx, tid, wave, lane);
    else role_S<5>(w, r, l, L, nr, cp, idx, tid, wave, lane);
  }
  else if (idx < 64) { if (wave < 4) role_V<true>(p, r, l, L, nr, cp, idx - 32, tid, wave, lane); else role_V<false>(p, r, l, L, nr, cp, idx - 32, tid, wave, lane); }
  else if (idx < 96) role_R(p, r, l, L, nr, (idx - 64) >> 4, (idx - 64) & 15, tid, wave, lane);
  else if (idx < 110) role_F1(p, r, l, L, nr, idx - 96, tid, wave, lane);
  else if (idx < 126) role_F2(p, r, l, L, nr, (idx - 110) >> 3, (idx - 110) & 7, tid, wave, lane);
  else role_N(p, r, l, L, nr, idx - 126, tid, wave, lane);
}

constexpr unsigned INIT_BLOCKS = 40 + (8 * NCOPY + 24) * 4;
__global__ void __launch_bounds__(256) rmt_init(const __hip_bfloat16* x, const __hip_bfloat16* mem0, unsigned char* ws) {
  const unsigned tid = __builtin_amdgcn_workitem_id_x(), bid = __builtin_amdgcn_workgroup_id_x();
  if (bid < 24) {
    if (tid < 64 && bid < NREG) {
      const rsrc_t r = __builtin_amdgcn_make_buffer_rsrc(ws, 0, WS_END, 0x00020000);
      unsigned best[2] = {~0u, ~0u};
      for (int i = 0; i < CAL_N; ++i) {
        UNR for (int hf = 0; hf < 2; ++hf) {
          const unsigned t0 = (unsigned)__builtin_amdgcn_s_memtime();
          __builtin_amdgcn_raw_buffer_store_b32(SENT, r, (tid & 31) * 4, (bid + 1) * 2 * MB - 2 * BLK + hf * BLK, 16);
          drain();
          best[hf] = umin(best[hf], (unsigned)__builtin_amdgcn_s_memtime() - t0);
        }
      }
      const unsigned xcc = __builtin_amdgcn_s_getreg(20 | (3 << 11)) & 7;
      if (tid == 0) *(unsigned*)(ws + WS_CAL + bid * 128) = ((best[1] < best[0] ? 1u : 0u) ^ (xcc >> 2)) | (xcc << 4);
    }
    return;
  }
  if (bid < 40) {
    const unsigned row = (bid - 24) * 4 + (tid >> 6), lane = tid & 63;
    const u32x4 v = *(const u32x4*)(x + row * DM + lane * 8);
    float s1 = 0.f, s2 = 0.f;
    UNR for (int j = 0; j < 4; ++j) { s1 += bflo(v[j]) + bfhi(v[j]); s2 += bflo(v[j]) * bflo(v[j]) + bfhi(v[j]) * bfhi(v[j]); }
    UNR for (int i = 0; i < 4; ++i) {
      s1 += dppf(s1, i); s2 += dppf(s2, i);
    }
    const float mean = xsum(s1) * (1.f / DM), rstd = __builtin_amdgcn_rsqf(__builtin_fmaxf(xsum(s2) * (1.f / DM) - mean * mean, 0.f) + 1e-5f);
    u32x4 o;
    UNR for (int j = 0; j < 4; ++j) o[j] = pk((bflo(v[j]) - mean) * rstd, (bfhi(v[j]) - mean) * rstd);
    UNR for (unsigned hf = 0; hf < 2; ++hf) *(u32x4*)(ws + ph(hf ? ~0u : 0u, V_XN + (row >> 5) * X_SLOT + (lane * 32 + (row & 31)) * 16)) = o;
  }
  else {
    const unsigned n = bid - 40, pair = n >> 2, o = (n & 3) * 4096 + tid * 16;
    u32x4 v = sent4();
    if (pair < 8 * NCOPY && (pair & 7) == 0) {
      const unsigned k = (o >> 4) & 511;
      const unsigned* src = (const unsigned*)(mem0 + (k & 7) * DM + (k >> 4) * 16) + ((k >> 3) & 1);
      v = unsent((u32x4){src[0], src[2], src[4], src[6]});
    }
    const unsigned base = pair < 8 * NCOPY ? pair * BLK : pair < 8 * NCOPY + 16 ? V_PM + (pair - 8 * NCOPY) * BLK : V_FL + (pair - 8 * NCOPY - 16) * BLK;
    *(u32x4*)(ws + 2 * base + o) = v;
  }
}

#ifdef NOPK_PUSHED
#pragma clang attribute pop
#endif

extern "C" void kernel_launch(void* const* d_in, const int* in_sizes, int n_in, void* d_out, int out_size, void* d_ws, size_t ws_size,
                              hipStream_t stream) {
  Params p;
  p.x = (const __hip_bfloat16*)d_in[0];
  p.Wq = (const __hip_bfloat16*)d_in[2]; p.Wk = (const __hip_bfloat16*)d_in[3]; p.Wv = (const __hip_bfloat16*)d_in[4];
  p.Wo = (const __hip_bfloat16*)d_in[5]; p.Wg = (const __hip_bfloat16*)d_in[8]; p.Wu = (const __hip_bfloat16*)d_in[9];
  p.Wd = (const __hip_bfloat16*)d_in[10];
  p.out = (__hip_bfloat16*)d_out; p.ws = (unsigned char*)(((size_t)d_ws + (2u << 20) - 1) & ~(size_t)((2u << 20) - 1));
  (void)hipFuncSetAttribute((const void*)rmt_mega, hipFuncAttributeMaxDynamicSharedMemorySize, LDS_BYTES);
  rmt_init<<<INIT_BLOCKS, 256, 0, stream>>>(p.x, (const __hip_bfloat16*)d_in[1], p.ws);
  rmt_mega<<<256, 512, LDS_BYTES, stream>>>(p);
}
```
